```python
import torch

def _softmax(x, axis=-1):
    return torch.softmax(x, dim=axis)
def _gelu(x):
    return torch.nn.functional.gelu(x, approximate="tanh")
def _silu(x):
    return torch.nn.functional.silu(x)
def _sigmoid(x):
    return torch.sigmoid(x)
def _layernorm(x, axis=-1, eps=1e-5):
    m = x.mean(dim=axis, keepdim=True)
    v = x.var(dim=axis, keepdim=True, unbiased=False)
    return (x - m) / torch.sqrt(v + eps)
def _scatter_add(tgt, idx, src, axis=0):
    return tgt.clone().index_add_(axis, idx, src)
def _segment_sum(src, seg, n):
    out = torch.zeros((n,) + src.shape[1:], dtype=src.dtype)
    return out.index_add_(0, seg, src)
def _pad(x, pw):
    flat = tuple(v for pair in reversed(pw) for v in pair)
    return torch.nn.functional.pad(x, flat)

kernel_name = 'gated_mix__linear_kernel_hedgehog__softmax_full_mha__prompt'

def setup_inputs():

    _w = (lambda a: (lambda t: t.to(torch.bfloat16) if t.is_floating_point() else t)(torch.as_tensor(a)))
    def _A_setup():
        B, S, D, H = 1, 2048, 640, 32
        Dh = D // H
        return {
            "x": _w((lambda *s: torch.randn(*s, dtype=torch.bfloat16) * 0.05)(B, S, D)),
            "wqkv": _w((lambda *s: torch.randn(*s, dtype=torch.bfloat16) * 1.6)(D, 3 * D)),
            "w_phi": _w((lambda *s: torch.randn(*s, dtype=torch.bfloat16) * 1.6)(H, Dh, Dh) * (Dh ** -0.5)),
            "wo": _w((lambda *s: torch.randn(*s, dtype=torch.bfloat16) * 0.05)(D, D)),
        }
    _A_in = _A_setup()

    def _B_setup():
        B, S, D = 1, 2048, 640
        return {
            "x": _w((lambda *s: torch.randn(*s, dtype=torch.bfloat16) * 0.05)(B, S, D)),
            "wqkv": _w((lambda *s: torch.randn(*s, dtype=torch.bfloat16) * 0.8)(D, 3 * D)),
            "wo": _w((lambda *s: torch.randn(*s, dtype=torch.bfloat16) * 0.05)(D, D)),
        }
    _B_in = _B_setup()

    return {
        "A_x": _A_in["x"],
        "A_wqkv": _A_in["wqkv"],
        "A_w_phi": _A_in["w_phi"],
        "A_wo": _A_in["wo"],
        "B_x": _B_in["x"],
        "B_wqkv": _B_in["wqkv"],
        "B_wo": _B_in["wo"],
        "gate_w": _w((lambda *s: torch.randn(*s, dtype=torch.bfloat16) * 0.05)(640) * 16.0),
    }

def reference(A_x, A_wqkv, A_w_phi, A_wo, B_x, B_wqkv, B_wo, gate_w):
    def _A_ref(x, wqkv, w_phi, wo):
        B, S, D = x.shape
        H = 32; Dh = D // H
        qkv = (lambda x, *d: x.permute(*d))((x @ wqkv).reshape(B, S, 3, H, Dh), 2, 0, 3, 1, 4)
        q, k, v = qkv[0], qkv[1], qkv[2]
        pq = torch.einsum("bhsd,hdf->bhsf", q, w_phi)
        pk = torch.einsum("bhsd,hdf->bhsf", k, w_phi)
        phi_q = torch.concatenate([_softmax(pq, axis=-1), _softmax(-pq, axis=-1)], axis=-1)
        phi_k = torch.concatenate([_softmax(pk, axis=-1), _softmax(-pk, axis=-1)], axis=-1)
        kv = torch.einsum("bhsf,bhsd->bhfd", phi_k, v)
        z  = torch.einsum("bhsf->bhf", phi_k)
        num = torch.einsum("bhqf,bhfd->bhqd", phi_q, kv)
        den = torch.einsum("bhqf,bhf->bhq", phi_q, z)[..., None] + 1e-6
        o = num / den
        return (lambda x, *d: x.permute(*d))(o, 0, 2, 1, 3).reshape(B, S, D) @ wo

    def _B_ref(x, wqkv, wo):
        B, S, D = x.shape
        H = 32; Dh = D // H
        qkv = (lambda x, *d: x.permute(*d))((x @ wqkv).reshape(B, S, 3, H, Dh), 2, 0, 3, 1, 4)
        q, k, v = qkv[0], qkv[1], qkv[2]
        scale = Dh ** -0.5
        scores = torch.einsum("bhqd,bhkd->bhqk", q, k) * scale
        p = _softmax(scores, axis=-1)
        o = torch.einsum("bhqk,bhkd->bhqd", p, v)
        return (lambda x, *d: x.permute(*d))(o, 0, 2, 1, 3).reshape(B, S, D) @ wo

    x = A_x
    g = _sigmoid((x * gate_w).sum(-1, keepdims=True))
    a = _A_ref(x, A_wqkv, A_w_phi, A_wo)
    b = _B_ref(x, B_wqkv, B_wo)
    return g * a + (1 - g) * b
```

```cpp
#include <hip/hip_runtime.h>
#include <hip/hip_bf16.h>
typedef unsigned short u16;
__device__ inline float b2f(u16 v){ return __uint_as_float(((unsigned)v)<<16); }
__device__ inline u16 f2b(float f){ unsigned u=__float_as_uint(f); u += 0x7fffu + ((u>>16)&1u); return (u16)(u>>16); }
__device__ inline float rb(float f){ return b2f(f2b(f)); }
constexpr int S=2048, D=640, H=32, DH=20;

__global__ void gemm_naive(const u16* A, const u16* B, float* C, int M, int N, int K){
  int n = blockIdx.x*blockDim.x+threadIdx.x, m = blockIdx.y;
  float acc=0; for(int k=0;k<K;k++) acc += b2f(A[m*K+k])*b2f(B[k*N+n]);
  C[m*N+n]=rb(acc);
}
__global__ void phi_k(const float* qkv, const u16* wphi, float* phi){
  int idx = blockIdx.x*blockDim.x+threadIdx.x;
  int which = idx&1, h=(idx>>1)&31, s=idx>>6;
  const float* q = qkv + s*1920 + which*640 + h*20;
  float p[20]; float mx=-1e30f, mn=1e30f;
  for(int f=0;f<20;f++){ float a=0; for(int d=0;d<20;d++) a+= q[d]*b2f(wphi[h*400+d*20+f]); a=rb(a); p[f]=a; mx=fmaxf(mx,a); mn=fminf(mn,a);}
  float s1=0,s2=0; for(int f=0;f<20;f++){ s1+=__expf(p[f]-mx); s2+=__expf(mn-p[f]); }
  float* o = phi + (size_t)which*S*H*40 + (s*H+h)*40;
  for(int f=0;f<20;f++){ o[f]=rb(__expf(p[f]-mx)/s1); o[20+f]=rb(__expf(mn-p[f])/s2); }
}
__global__ void kv_k(const float* qkv, const float* phi, float* kv){
  int h=blockIdx.x; int t=threadIdx.x; if(t>=840) return; int f=t/21, d=t%21;
  const float* pk = phi + (size_t)S*H*40;
  float a=0; for(int s=0;s<S;s++) a += pk[(s*H+h)*40+f]*(d<20? qkv[s*1920+1280+h*20+d]:1.f);
  kv[h*840+t]=rb(a);
}
__global__ void oa_k(const float* phi, const float* kv, u16* o){
  int idx = blockIdx.x*blockDim.x+threadIdx.x; int h=idx&31, s=idx>>5;
  const float* pq = phi + (s*H+h)*40; const float* k = kv+h*840;
  float den=0; for(int f=0;f<40;f++) den+=pq[f]*k[f*21+20]; den = rb(rb(den)+1e-6f);
  for(int d=0;d<20;d++){ float a=0; for(int f=0;f<40;f++) a+=pq[f]*k[f*21+d]; o[s*640+h*20+d]=f2b(rb(a)/den);}
}
__global__ void attn_k(const float* qkv, u16* o){
  int s = blockIdx.x*blockDim.x+threadIdx.x, h=blockIdx.y;
  float q[20], acc[20]; for(int d=0;d<20;d++){ q[d]=qkv[s*1920+h*20+d]*0.22360679775f; acc[d]=0; }
  float m=-1e30f,l=0;
  for(int j=0;j<S;j++){ float a=0; for(int d=0;d<20;d++) a+=q[d]*qkv[j*1920+640+h*20+d];
    float mn=fmaxf(m,a); float c=__expf(m-mn), p=__expf(a-mn); l=l*c+p; for(int d=0;d<20;d++) acc[d]=acc[d]*c+p*qkv[j*1920+1280+h*20+d]; m=mn; }
  for(int d=0;d<20;d++) o[s*640+h*20+d]=f2b(acc[d]/l);
}
__global__ void fin_k(const u16* x, const u16* gw, const float* a, const float* b, u16* out){
  int s=blockIdx.x; __shared__ float g;
  if(threadIdx.x==0){ float t=0; for(int k=0;k<640;k++) t+=rb(b2f(x[s*640+k])*b2f(gw[k])); t=rb(t); g=rb(1.f/(1.f+__expf(-t))); }
  __syncthreads();
  int n=threadIdx.x; out[s*640+n]=f2b(rb(g*a[s*640+n])+rb(rb(1.f-g)*b[s*640+n]));
}
extern "C" void kernel_launch(void* const* d_in, const int* in_sizes, int n_in, void* d_out, int out_size, void* d_ws, size_t ws_size, hipStream_t stream){
  const u16* x=(const u16*)d_in[0]; const u16* wA=(const u16*)d_in[1]; const u16* wphi=(const u16*)d_in[2]; const u16* woA=(const u16*)d_in[3];
  const u16* wB=(const u16*)d_in[5]; const u16* woB=(const u16*)d_in[6]; const u16* gw=(const u16*)d_in[7];
  float* w=(float*)d_ws; float* qa=w; float* qb=qa+S*1920; float* phi=qb+S*1920; float* kv=phi+2*S*H*40; float* a=kv+32*840; float* b=a+S*640; u16* oa=(u16*)(b+S*640); u16* ob=oa+S*640;
  gemm_naive<<<dim3(1920/128,S),128,0,stream>>>(x,wA,qa,S,1920,640);
  gemm_naive<<<dim3(1920/128,S),128,0,stream>>>(x,wB,qb,S,1920,640);
  phi_k<<<S*64/256,256,0,stream>>>(qa,wphi,phi);
  kv_k<<<32,896,0,stream>>>(qa,phi,kv);
  oa_k<<<S*32/256,256,0,stream>>>(phi,kv,oa);
  attn_k<<<dim3(S/64,H),64,0,stream>>>(qb,ob);
  gemm_naive<<<dim3(640/128,S),128,0,stream>>>(oa,woA,a,S,640,640);
  gemm_naive<<<dim3(640/128,S),128,0,stream>>>(ob,woB,b,S,640,640);
  fin_k<<<S,640,0,stream>>>(x,gw,a,b,(u16*)d_out);
}
```

```cpp
#include <hip/hip_runtime.h>
#include <stdint.h>

typedef unsigned short u16;
typedef __attribute__((ext_vector_type(8))) short bf16x8;
typedef __attribute__((ext_vector_type(4))) short s16x4;
typedef __attribute__((ext_vector_type(16))) float f32x16;
typedef __attribute__((ext_vector_type(4))) float f32x4;
typedef __attribute__((ext_vector_type(4))) unsigned u32x4;
typedef __attribute__((ext_vector_type(2))) unsigned u32x2;
typedef u32x4 u32x4_a8 __attribute__((aligned(8)));
typedef __attribute__((ext_vector_type(2))) __bf16 bf16x2_t;
typedef __attribute__((ext_vector_type(2))) float f32x2;
#define LAS __attribute__((address_space(3)))
#define GAS __attribute__((address_space(1)))
typedef LAS const char* lds_cptr;
typedef LAS char* lds_ptr;
#define DI __device__ __forceinline__
#define SBAR() __builtin_amdgcn_sched_barrier(0)
#define PIN(x) asm volatile("" : "+v"(x))
#define MFMA32(a, b, c) __builtin_amdgcn_mfma_f32_32x32x16_bf16(a, b, c, 0, 0, 0)
#define MFMA16(a, b, c) __builtin_amdgcn_mfma_f32_16x16x32_bf16(a, b, c, 0, 0, 0)
#define WAIT_BAR(N) asm volatile("s_waitcnt vmcnt(" #N ") lgkmcnt(0)\n\ts_barrier" ::: "memory")

constexpr int S = 2048, D = 640, H = 32;
constexpr int TQ = 1280;
constexpr int TV = 1344;
constexpr int TKV = TQ + TV;
constexpr size_t QIMG = (size_t)H * 64 * TQ, KVIMG = (size_t)H * 64 * TKV;
constexpr size_t WS_QA = 0, WS_QB = WS_QA + QIMG, WS_KVA = WS_QB + QIMG, WS_KVB = WS_KVA + KVIMG;
constexpr size_t WS_G = WS_KVB + KVIMG;
constexpr size_t WS_PHI = WS_G + 8192;
constexpr size_t WS_CNT = WS_PHI + 32 * 2048;
constexpr size_t WS_SLAB = WS_CNT + 32 * 256;
constexpr size_t WS_OA = WS_SLAB + (size_t)32 * 8 * 6144;
constexpr size_t WS_OB = WS_OA + (size_t)16 * 2048 * 80;
constexpr float QSCALE = 0.22360679774997896f * 1.4426950408889634f;
constexpr float LOG2E = 1.4426950408889634f;

#define STAMP(k, i) do {} while (0)
DI unsigned pk2(float lo, float hi) {
  f32x2 v = {lo, hi};
  return __builtin_bit_cast(unsigned, __builtin_convertvector(v, bf16x2_t));
}
DI float b2f(u16 v) { return __uint_as_float(((unsigned)v) << 16); }
DI void glds16(const void* base, unsigned voff, unsigned lds_dst) {
  unsigned keep;
  asm volatile("s_mov_b32 %0, m0\n\ts_mov_b32 m0, %3\n\ts_nop 0\n\tglobal_load_lds_dwordx4 %1, %2\n\ts_mov_b32 m0, %0"
               : "=&s"(keep) : "v"(voff), "s"(base), "s"(lds_dst) : "memory");
}
DI s16x4 ldtr(lds_cptr p) {
  typedef short v4i16_t __attribute__((ext_vector_type(4)));
  return __builtin_bit_cast(s16x4, __builtin_amdgcn_ds_read_tr16_b64_v4i16((LAS v4i16_t*)p));
}
DI bf16x8 cat8(s16x4 a, s16x4 b) { return __builtin_shufflevector(a, b, 0, 1, 2, 3, 4, 5, 6, 7); }
DI float swap32(float x) {
  auto rr = __builtin_amdgcn_permlane32_swap(__float_as_uint(x), __float_as_uint(x), false, false);
  return __uint_as_float((threadIdx.x & 32) ? rr[0] : rr[1]);
}
DI float max_halves(float x) {
  auto rr = __builtin_amdgcn_permlane32_swap(__float_as_uint(x), __float_as_uint(x), false, false);
  return __builtin_fmaxf(__uint_as_float(rr[0]), __uint_as_float(rr[1]));
}
DI float sum_halves(float x) {
  auto rr = __builtin_amdgcn_permlane32_swap(__float_as_uint(x), __float_as_uint(x), false, false);
  return __uint_as_float(rr[0]) + __uint_as_float(rr[1]);
}

constexpr int K1_SLOT = 49152, K1_LDS = 3 * K1_SLOT;
template <bool ISV>
DI void k1_loop(f32x16 (&acc)[2][2], const char* xb, const char* wb, unsigned xo0, unsigned xo1, unsigned wo, unsigned lds0, int wid,
                lds_cptr lx, lds_cptr lw0, lds_cptr lw1, unsigned sw, char* ws) {
  auto stage = [&](int kt) {
    const unsigned dst = lds0 + (kt % 3) * K1_SLOT;
    const char* xs = xb + kt * 128;
    const char* ws = wb + (size_t)kt * 64 * 3840;
    glds16(xs, xo0, dst + wid * 2048);
    glds16(xs, xo1, dst + wid * 2048 + 1024);
#pragma unroll
    for (int i = 0; i < 4; ++i) glds16(ws + i * 4 * 3840, wo, dst + 16384 + wid * 4096 + i * 1024);
  };
  stage(0); stage(1);
#pragma unroll
  for (int kt = 0; kt < 10; ++kt) {
    if (kt < 9) WAIT_BAR(6); else WAIT_BAR(0);
    if (kt + 2 < 10) stage(kt + 2);
    const int so = (kt % 3) * K1_SLOT;
#pragma unroll
    for (int ks = 0; ks < 4; ++ks) {
      bf16x8 xf[2], wf[2];
#pragma unroll
      for (int tm = 0; tm < 2; ++tm) xf[tm] = *(LAS const bf16x8*)(lx + so + tm * 4096 + ((ks * 32) ^ sw));
      wf[0] = cat8(ldtr(lw0 + so + ks * 4096), ldtr(lw0 + so + ks * 4096 + 1024));
      wf[1] = cat8(ldtr(lw1 + so + ks * 4096), ldtr(lw1 + so + ks * 4096 + 1024));
#pragma unroll
      for (int tn = 0; tn < 2; ++tn)
#pragma unroll
        for (int tm = 0; tm < 2; ++tm)
          acc[tn][tm] = ISV ? MFMA32(xf[tm], wf[tn], acc[tn][tm]) : MFMA32(wf[tn], xf[tm], acc[tn][tm]);
    }
  }
}

__global__ __launch_bounds__(512, 2) void k1_qkv(const u16* __restrict__ x, const u16* __restrict__ wA, const u16* __restrict__ wB,
                                                  const u16* __restrict__ wphi, const u16* __restrict__ gw, char* __restrict__ ws) {
  extern __shared__ __attribute__((aligned(16))) char lds[];
  const int tid = threadIdx.x, lane = tid & 63, r = lane & 31, h = lane >> 5;
  const int wid = __builtin_amdgcn_readfirstlane(tid >> 6);
  const int xcd = blockIdx.x & 7, idx = blockIdx.x >> 3;
  STAMP(0, 0);
  if (idx >= 30) {
    const int a = (idx - 30) * 8 + xcd;
    float* g = (float*)(ws + WS_G);
    const int l16 = lane & 15;
#pragma unroll
    for (int it = 0; it < 4; ++it) {
      const int row = a * 128 + wid * 16 + it * 4 + (lane >> 4);
      float sum = 0.f;
#pragma unroll
      for (int c = 0; c < 5; ++c) {
        const bf16x8 xv = *(const bf16x8*)(x + row * D + (c * 16 + l16) * 8);
        const bf16x8 gv = *(const bf16x8*)(gw + (c * 16 + l16) * 8);
#pragma unroll
        for (int j = 0; j < 8; ++j) sum += b2f((u16)xv[j]) * b2f((u16)gv[j]);
      }
#pragma unroll
      for (int o = 1; o < 16; o <<= 1) sum += __shfl_xor(sum, o);
      if (l16 == 0) g[row] = 1.f / (1.f + __expf(-sum));
    }
    const u32x4 ones = {0x3F803F80u, 0x3F803F80u, 0x3F803F80u, 0x3F803F80u};
    {
      const int e = a * 512 + tid;
      const size_t off = (size_t)(e >> 2) * TKV + TQ + ((e & 3) * 21 + 20) * 16;
      *(u32x4*)(ws + WS_KVA + off) = ones;
      *(u32x4*)(ws + WS_KVB + off) = ones;
    }
    if (tid < 256) {
      const int head = a * 2 + (tid >> 7), m = (tid >> 6) & 1;
      const int hh = (r >> 2) & 1, reg = (r & 3) + 4 * (r >> 3);
      bf16x8 v;
#pragma unroll
      for (int j = 0; j < 8; ++j) {
        const int d = m == 0 ? 8 * h + j : 16 + j;
        const bool ok = reg < 10 && (m == 0 || (h == 0 && j < 4));
        v[j] = ok ? (short)wphi[head * 400 + d * 20 + 10 * hh + reg] : (short)0;
      }
      *(bf16x8*)(ws + WS_PHI + head * 2048 + m * 1024 + lane * 16) = v;
    }
    if (a == 0 && tid < 32) *(unsigned*)(ws + WS_CNT + tid * 256) = 0u;
    return;
  }
  const int lin = xcd * 30 + idx, rem = lin % 120, col = rem >> 3, row = (lin / 120) * 8 + (rem & 7);
  const int wr = wid >> 2, wc = wid & 3;
  const unsigned lds0 = (unsigned)(uintptr_t)lds;
  const char* xb = (const char*)x + (size_t)row * 128 * 1280;
  const unsigned xo0 = (unsigned)((wid * 16 + (lane >> 3)) * 1280 + (((lane & 7) ^ ((lane >> 4) & 7)) * 16));
  const unsigned xo1 = (unsigned)((wid * 16 + 8 + (lane >> 3)) * 1280 + (((lane & 7) ^ ((4 + (lane >> 4)) & 7)) * 16));
  const int dcol = col * 256 + (wid >> 2) * 128;
  const char* wb = dcol < 1920 ? (const char*)wA + dcol * 2 : (const char*)wB + (dcol - 1920) * 2;
  const unsigned wo = (unsigned)(((wid & 3) * 16 + (lane >> 4)) * 3840 + (((lane & 15) ^ ((lane >> 4) << 2)) * 16));
  const int xr = wr * 64 + r;
  const unsigned swz = (xr >> 1) & 7;
  const lds_cptr lx = (lds_cptr)lds + xr * 128 + ((h ^ (swz & 1)) * 16);
  const unsigned sw = (swz & 6) * 16;
  const int q4 = (lane & 15) >> 2, p4 = lane & 3, blk = (lane >> 4) & 1;
  const lds_cptr lwb = (lds_cptr)lds + 16384 + (wc >> 1) * 16384 + (8 * h + q4) * 256 + blk * 32 + p4 * 8;
  const lds_cptr lw0 = lwb + ((((wc & 1) * 2) ^ q4) * 64), lw1 = lwb + ((((wc & 1) * 2 + 1) ^ q4) * 64);
  const int gcol = col * 256 + wc * 64, br = gcol >= 1920, within = gcol - br * 1920, type = within / 640, hcol = within - type * 640;
  f32x16 acc[2][2];
#pragma unroll
  for (int i = 0; i < 2; ++i)
#pragma unroll
    for (int j = 0; j < 2; ++j) acc[i][j] = f32x16{};
  const int T0 = row * 4 + wr * 2;
  if (type == 2) {
    k1_loop<true>(acc, xb, wb, xo0, xo1, wo, lds0, wid, lx, lw0, lw1, sw, ws);
    STAMP(0, 1);
    char* kv = ws + (br ? WS_KVB : WS_KVA);
#pragma unroll
    for (int tn = 0; tn < 2; ++tn) {
      const int n = hcol + 32 * tn + r, head = n / 20, d = n - head * 20, c = d >> 2;
      const int rho = ((c < 3 ? 2 * c : 2 * c - 5) << 2) + (d & 3);
      char* dst = kv + (size_t)(head * 64 + T0) * TKV + TQ + (h * 21 + rho) * 16;
#pragma unroll
      for (int tm = 0; tm < 2; ++tm)
#pragma unroll
        for (int s = 0; s < 2; ++s) {
          const f32x16& a = acc[tn][tm];
          u32x4 v = {pk2(a[8 * s], a[8 * s + 1]), pk2(a[8 * s + 2], a[8 * s + 3]), pk2(a[8 * s + 4], a[8 * s + 5]), pk2(a[8 * s + 6], a[8 * s + 7])};
          *(u32x4*)(dst + tm * TKV + s * 672) = v;
        }
    }
  } else {
    k1_loop<false>(acc, xb, wb, xo0, xo1, wo, lds0, wid, lx, lw0, lw1, sw, ws);
    STAMP(0, 1);
    char* img = ws + (type == 0 ? (br ? WS_QB : WS_QA) : (br ? WS_KVB : WS_KVA));
    const int ts = type == 0 ? TQ : TKV;
    const float sc = (type == 0 && br) ? QSCALE : 1.f;
#pragma unroll
    for (int tn = 0; tn < 2; ++tn)
#pragma unroll
      for (int rq = 0; rq < 4; ++rq) {
        const int qd = (hcol >> 2) + 8 * tn + 2 * rq + h, head = qd / 5, c = qd - head * 5;
        char* dst = img + (size_t)(head * 64 + T0) * ts + (c < 4 ? (c >> 1) * 512 + r * 16 + (c & 1) * 8 : 1024 + r * 8);
#pragma unroll
        for (int tm = 0; tm < 2; ++tm) {
          const f32x16& a = acc[tn][tm];
          u32x2 v = {pk2(a[4 * rq] * sc, a[4 * rq + 1] * sc), pk2(a[4 * rq + 2] * sc, a[4 * rq + 3] * sc)};
          *(u32x2*)(dst + tm * ts) = v;
        }
      }
  }
  STAMP(0, 2);
}

constexpr int NS = 4, STG = 8 * TKV;
constexpr int K2_PHIK = NS * STG, K2_MRG = K2_PHIK, K2_KVB = K2_MRG + 28672, K2_LDS = K2_KVB + 3072;

DI void featmap(const bf16x8 f1, const bf16x8 f2, const bf16x8 x1, const bf16x8 x2, u32x4 (&pk)[3]) {
  f32x16 y = MFMA32(f1, x1, f32x16{});
  y = MFMA32(f2, x2, y);
  float mx = y[0], mn = y[0];
#pragma unroll
  for (int i = 1; i < 10; ++i) { mx = __builtin_fmaxf(mx, y[i]); mn = __builtin_fminf(mn, y[i]); }
  mx = max_halves(mx); mn = -max_halves(-mn);
  const float a = -mx * LOG2E, b = mn * LOG2E;
  float ep[10], em[10], sp = 0.f, sm = 0.f;
#pragma unroll
  for (int i = 0; i < 10; ++i) {
    ep[i] = __builtin_amdgcn_exp2f(__builtin_fmaf(y[i], LOG2E, a)); sp += ep[i];
    em[i] = __builtin_amdgcn_exp2f(__builtin_fmaf(y[i], -LOG2E, b)); sm += em[i];
  }
  const float rp = __builtin_amdgcn_rcpf(sum_halves(sp)), rm = __builtin_amdgcn_rcpf(sum_halves(sm));
#pragma unroll
  for (int i = 0; i < 4; ++i) pk[0][i] = pk2(ep[2 * i] * rp, ep[2 * i + 1] * rp);
  pk[1][0] = pk2(ep[8] * rp, ep[9] * rp);
#pragma unroll
  for (int i = 0; i < 3; ++i) pk[1][1 + i] = pk2(em[2 * i] * rm, em[2 * i + 1] * rm);
  pk[2][0] = pk2(em[6] * rm, em[7] * rm); pk[2][1] = pk2(em[8] * rm, em[9] * rm); pk[2][2] = 0u; pk[2][3] = 0u;
}
DI float rowmax16(const f32x16& s) {
  float a = __builtin_fmaxf(__builtin_fmaxf(s[0], s[1]), s[2]);
#pragma unroll
  for (int i = 3; i < 15; i += 2) a = __builtin_fmaxf(__builtin_fmaxf(a, s[i]), s[i + 1]);
  a = __builtin_fmaxf(a, s[15]);
  return max_halves(a);
}
DI void store_o(char* dst, const float (&o)[12], int h) {
  u32x4 v = {pk2(o[0], o[1]), pk2(o[2], o[3]), pk2(o[4], o[5]), pk2(o[6], o[7])};
  *(u32x4_a8*)(dst + h * 24) = v;
  if (h == 0) { u32x2 w = {pk2(o[8], o[9]), pk2(o[10], o[11])}; *(u32x2*)(dst + 16) = w; }
}

__global__ __launch_bounds__(512, 2) void k2_attn(char* __restrict__ ws) {
  extern __shared__ __attribute__((aligned(16))) char lds[];
  const int tid = threadIdx.x, lane = tid & 63, r = lane & 31, h = lane >> 5;
  const int wid = __builtin_amdgcn_readfirstlane(tid >> 6);
  const int head = (blockIdx.x & 7) * 4 + ((blockIdx.x >> 3) & 3), qb = blockIdx.x >> 5;
  const int qp = wid & 3, kh = wid >> 2;
  const unsigned lds0 = (unsigned)(uintptr_t)lds;
  const char* kvg = ws + WS_KVB + (size_t)head * 64 * TKV;
  const unsigned do0 = (unsigned)tid * 16, do2 = 16384u + (unsigned)(wid * 36 + lane) * 16;
  auto dma = [&](int st) {
    const char* src = kvg + st * STG;
    const unsigned dst = lds0 + (st % NS) * STG;
    glds16(src, do0, dst + wid * 1024);
    glds16(src + 8192, do0, dst + 8192 + wid * 1024);
    if (lane < 36) glds16(src, do2, dst + 16384 + wid * 576);
  };
  STAMP(1, 0);
  dma(0); dma(1); dma(2);
  const int tA = qb * 8 + qp * 2, own = tA + kh;
  const unsigned fo1 = h * 512 + r * 16, fo2 = 1024 + r * 8;
  const char* qg = ws + WS_QB + (size_t)(head * 64 + tA) * TQ;
  bf16x8 q1[2], q2[2];
#pragma unroll
  for (int i = 0; i < 2; ++i) {
    q1[i] = *(const bf16x8*)(qg + i * TQ + fo1);
    const u32x2 t = *(const u32x2*)(qg + i * TQ + fo2);
    u32x4 u = {h ? 0u : t[0], h ? 0u : t[1], 0u, 0u};
    q2[i] = __builtin_bit_cast(bf16x8, u);
  }
  const float gate = ((const float*)(ws + WS_G))[own * 32 + r];
  u32x4 phq[3];
  {
    const bf16x8 f1 = *(const bf16x8*)(ws + WS_PHI + head * 2048 + lane * 16), f2 = *(const bf16x8*)(ws + WS_PHI + head * 2048 + 1024 + lane * 16);
    const char* qa = ws + WS_QA + (size_t)(head * 64 + own) * TQ;
    const char* ka = ws + WS_KVA + (size_t)(head * 64 + own) * TKV;
    const bf16x8 xq1 = *(const bf16x8*)(qa + fo1), xk1 = *(const bf16x8*)(ka + fo1);
    const u32x2 tq = *(const u32x2*)(qa + fo2), tk = *(const u32x2*)(ka + fo2);
    const u32x4 uq = {tq[0], tq[1], 0u, 0u}, uk = {tk[0], tk[1], 0u, 0u};
    bf16x8 va[8];
    const int dt = wid & 1, ft = wid >> 1, g4 = lane >> 4, l16 = lane & 15;
    if (wid < 6) {
      const int slot = dt * 16 + l16 < 20 ? dt * 16 + l16 : 20;
      const char* vsrc = ws + WS_KVA + (size_t)(head * 64 + qb * 8) * TKV + TQ + (g4 * 21 + slot) * 16;
#pragma unroll
      for (int t = 0; t < 8; ++t) va[t] = *(const bf16x8*)(vsrc + t * TKV);
    }
    u32x4 phk[3];
    featmap(f1, f2, xk1, __builtin_bit_cast(bf16x8, uk), phk);
    const lds_ptr pw = (lds_ptr)lds + K2_PHIK + ((qp * 2 + kh) * 32 + r) * 96 + h * 16;
#pragma unroll
    for (int i = 0; i < 3; ++i) *(LAS u32x4*)(pw + i * 32) = phk[i];
    featmap(f1, f2, xq1, __builtin_bit_cast(bf16x8, uq), phq);
    asm volatile("s_waitcnt lgkmcnt(0)\n\ts_barrier" ::: "memory");
    if (wid < 6) {
      const lds_cptr pr = (lds_cptr)lds + K2_PHIK + (16 * (g4 >> 1) + 4 * (g4 & 1) + (l16 >> 2)) * 96 + ft * 32 + (l16 & 3) * 8;
      f32x4 kva = {};
#pragma unroll
      for (int t = 0; t < 8; ++t) kva = MFMA16(va[t], cat8(ldtr(pr + t * 32 * 96), ldtr(pr + t * 32 * 96 + 8 * 96)), kva);
      char* sl = ws + WS_SLAB + (size_t)(head * 8 + qb) * 6144 + wid * 1024 + lane * 16;
      asm volatile("s_nop 7\n\tglobal_store_dwordx4 %0, %1, off sc1\n\ts_nop 1\n\ts_waitcnt vmcnt(0)" ::"v"(sl), "v"(kva) : "memory");
      if (lane == 0) __hip_atomic_fetch_add((GAS unsigned*)(ws + WS_CNT + head * 256), 1u, __ATOMIC_RELAXED, __HIP_MEMORY_SCOPE_AGENT);
    }
  }
  STAMP(1, 1);
  const int vslot = r < 20 ? r : 20;
  const lds_cptr lk1 = (lds_cptr)lds + kh * 4 * TKV + fo1, lk2 = (lds_cptr)lds + kh * 4 * TKV + fo2;
  const lds_cptr lv = (lds_cptr)lds + kh * 4 * TKV + TQ + (h * 21 + vslot) * 16;
  WAIT_BAR(0);
  bf16x8 k1, v0, v1;
  u32x4 k2 = {0u, 0u, 0x3F80u, 0u};
  f32x16 o[2], sa, sb;
  o[0] = f32x16{}; o[1] = f32x16{};
  float mh[2];
  k1 = *(LAS const bf16x8*)(lk1);
  { const u32x2 t = *(LAS const u32x2*)(lk2); k2[0] = t[0]; k2[1] = t[1]; }
#pragma unroll
  for (int i = 0; i < 2; ++i) {
    f32x16 s = MFMA32(k1, q1[i], f32x16{});
    s = MFMA32(__builtin_bit_cast(bf16x8, k2), q2[i], s);
    const unsigned mb = pk2(-rowmax16(s), 0.f) & 0xFFFFu;
    mh[i] = -__uint_as_float(mb << 16);
    u32x4 u = __builtin_bit_cast(u32x4, q2[i]);
    u[2] = h ? 0u : mb;
    q2[i] = __builtin_bit_cast(bf16x8, u);
  }
  sa = MFMA32(k1, q1[0], f32x16{});
  sa = MFMA32(__builtin_bit_cast(bf16x8, k2), q2[0], sa);
  v0 = *(LAS const bf16x8*)(lv);
  v1 = bf16x8{};
  u32x4 p0, p1 = {0u, 0u, 0u, 0u};
  sb = f32x16{};
#define EX4(Sx, i) do { Sx[i] = __builtin_amdgcn_exp2f(Sx[i]); Sx[i + 1] = __builtin_amdgcn_exp2f(Sx[i + 1]); Sx[i + 2] = __builtin_amdgcn_exp2f(Sx[i + 2]); Sx[i + 3] = __builtin_amdgcn_exp2f(Sx[i + 3]); } while (0)
#define BF(x) __builtin_bit_cast(bf16x8, x)
#define PVM(a, b, c) MFMA32(a, b, c)
#define QKM(a, b, c, n) MFMA32(a, b, c)
#define UNIT(C, N, qc, qn, L0, L1, L2, L3) do { \
    p1[2] = pk2(N[12], N[13]); p1[3] = pk2(N[14], N[15]); PIN(p1); SBAR(); \
    N = QKM(k1, q1[qn], f32x16{}, N); SBAR(); L0; EX4(C, 0); PIN(C); SBAR(); \
    o[qn] = PVM(v1, BF(p1), o[qn]); SBAR(); L1; p0[0] = pk2(C[0], C[1]); p0[1] = pk2(C[2], C[3]); EX4(C, 4); PIN(C); PIN(p0); SBAR(); \
    N = QKM(BF(k2), q2[qn], N, N); SBAR(); L2; p0[2] = pk2(C[4], C[5]); p0[3] = pk2(C[6], C[7]); EX4(C, 8); PIN(C); PIN(p0); SBAR(); \
    o[qc] = PVM(v0, BF(p0), o[qc]); SBAR(); L3; p1[0] = pk2(C[8], C[9]); p1[1] = pk2(C[10], C[11]); EX4(C, 12); PIN(C); PIN(p1); SBAR(); \
  } while (0)
#define TILE(cur, nxt) do { \
    UNIT(sa, sb, 0, 1, k1 = *(LAS const bf16x8*)(lk1 + (nxt)), v1 = *(LAS const bf16x8*)(lv + (cur) + 672), \
         { const u32x2 t_ = *(LAS const u32x2*)(lk2 + (nxt)); k2[0] = t_[0]; k2[1] = t_[1]; }, (void)0); \
    UNIT(sb, sa, 1, 0, (void)0, (void)0, (void)0, v0 = *(LAS const bf16x8*)(lv + (nxt))); \
  } while (0)
  int sc = 0;
  STAMP(1, 2);
  for (int j = 0; j < 8; ++j) {
    const int sn = sc + STG == NS * STG ? 0 : sc + STG;
    TILE(sc, sc + TKV);
    TILE(sc + TKV, sc + 2 * TKV);
    TILE(sc + 2 * TKV, sc + 3 * TKV);
    if (j < 6) WAIT_BAR(3); else WAIT_BAR(0);
    if (j + 3 < 8) dma(j + 3);
    TILE(sc + 3 * TKV, sn);
    sc = sn;
  }
  STAMP(1, 3);
  p1[2] = pk2(sb[12], sb[13]); p1[3] = pk2(sb[14], sb[15]);
  o[1] = MFMA32(v1, BF(p1), o[1]);
  {
    const f32x16& og = kh ? o[0] : o[1];
    const lds_ptr mw = (lds_ptr)lds + K2_MRG + wid * 3584 + lane * 4;
#pragma unroll
    for (int i = 0; i < 13; ++i) *(LAS float*)(mw + i * 256) = og[i];
    *(LAS float*)(mw + 13 * 256) = kh ? mh[0] : mh[1];
  }
  if (wid == 0) {
    GAS unsigned* cnt = (GAS unsigned*)(ws + WS_CNT + head * 256);
    for (unsigned spins = 0; spins < (1u << 22); ++spins) {
      if ((unsigned)__builtin_amdgcn_readfirstlane(__hip_atomic_load(cnt, __ATOMIC_RELAXED, __HIP_MEMORY_SCOPE_AGENT)) >= 48u) break;
      __builtin_amdgcn_s_sleep(1);
    }
  }
  asm volatile("s_waitcnt lgkmcnt(0)\n\ts_barrier" ::: "memory");
  f32x4 part[8];
  if (wid < 6) {
    const char* sl = ws + WS_SLAB + (size_t)head * 8 * 6144 + wid * 1024 + lane * 16;
#pragma unroll
    for (int i = 0; i < 8; ++i) asm volatile("global_load_dwordx4 %0, %1, off sc1" : "=v"(part[i]) : "v"(sl + i * 6144) : "memory");
  }
  float ob[12];
  bool bad;
  {
    const f32x16& om = kh ? o[1] : o[0];
    const float m1 = kh ? mh[1] : mh[0];
    const lds_cptr mr = (lds_cptr)lds + K2_MRG + (wid ^ 4) * 3584 + lane * 4;
    const float m2 = *(LAS const float*)(mr + 13 * 256);
    const float mm = __builtin_fmaxf(m1, m2), f1 = __builtin_amdgcn_exp2f(m1 - mm), f2 = __builtin_amdgcn_exp2f(m2 - mm);
    const float l1 = om[12], l2 = *(LAS const float*)(mr + 12 * 256);
    const float l = l1 * f1 + l2 * f2;
    bad = !(l1 < 1e30f) || !(l2 < 1e30f);
    const float sc2 = (1.f - gate) / l;
#pragma unroll
    for (int i = 0; i < 12; ++i) {
      ob[i] = (om[i] * f1 + *(LAS const float*)(mr + i * 256) * f2) * sc2;
      if (i < 8 || h == 0) bad = bad || !(__builtin_fabsf(ob[i]) < 3e38f);
    }
  }
  if (__any(bad)) {
    const bf16x8 qq1 = kh ? q1[1] : q1[0];
    u32x4 uq2 = __builtin_bit_cast(u32x4, kh ? q2[1] : q2[0]); uq2[2] = 0u;
    f32x16 oo = {};
    float m = -INFINITY;
    for (int t = 0; t < 64; ++t) {
      const char* tb = kvg + t * TKV;
      const bf16x8 a1 = *(const bf16x8*)(tb + fo1);
      const u32x2 tt = *(const u32x2*)(tb + fo2);
      const u32x4 a2 = {tt[0], tt[1], 0u, 0u};
      const bf16x8 w0 = *(const bf16x8*)(tb + TQ + (h * 21 + vslot) * 16), w1 = *(const bf16x8*)(tb + TQ + 672 + (h * 21 + vslot) * 16);
      f32x16 s = MFMA32(a1, qq1, f32x16{});
      s = MFMA32(BF(a2), BF(uq2), s);
      const float mn = __builtin_fmaxf(m, rowmax16(s)), al = __builtin_amdgcn_exp2f(m - mn);
      m = mn;
#pragma unroll
      for (int i = 0; i < 16; ++i) { oo[i] *= al; s[i] = __builtin_amdgcn_exp2f(s[i] - mn); }
      const u32x4 pa = {pk2(s[0], s[1]), pk2(s[2], s[3]), pk2(s[4], s[5]), pk2(s[6], s[7])};
      const u32x4 pb = {pk2(s[8], s[9]), pk2(s[10], s[11]), pk2(s[12], s[13]), pk2(s[14], s[15])};
      oo = MFMA32(w0, BF(pa), oo);
      oo = MFMA32(w1, BF(pb), oo);
    }
    const float sc2 = (1.f - gate) / oo[12];
#pragma unroll
    for (int i = 0; i < 12; ++i) ob[i] = oo[i] * sc2;
  }
  const size_t orec = ((size_t)(head >> 1) * S + own * 32 + r) * 80 + (head & 1) * 40;
  store_o(ws + WS_OB + orec, ob, h);
  if (wid < 6) {
    asm volatile("s_waitcnt vmcnt(0)" : "+v"(part[0]), "+v"(part[1]), "+v"(part[2]), "+v"(part[3]), "+v"(part[4]), "+v"(part[5]), "+v"(part[6]), "+v"(part[7])::"memory");
    f32x4 t = ((part[0] + part[1]) + (part[2] + part[3])) + ((part[4] + part[5]) + (part[6] + part[7]));
    const lds_ptr kw = (lds_ptr)lds + K2_KVB + ((wid & 1) * 16 + (lane >> 4) * 4) * 96 + ((wid >> 1) * 16 + (lane & 15)) * 2;
    const unsigned a = pk2(t[0], t[1]), b = pk2(t[2], t[3]);
    *(LAS u16*)(kw) = (u16)a; *(LAS u16*)(kw + 96) = (u16)(a >> 16); *(LAS u16*)(kw + 192) = (u16)b; *(LAS u16*)(kw + 288) = (u16)(b >> 16);
  }
  asm volatile("s_waitcnt lgkmcnt(0)\n\ts_barrier" ::: "memory");
  {
    const lds_cptr kr = (lds_cptr)lds + K2_KVB + vslot * 96 + h * 16;
    f32x16 n = {};
#pragma unroll
    for (int i = 0; i < 3; ++i) n = MFMA32(*(LAS const bf16x8*)(kr + i * 32), BF(phq[i]), n);
    const float sc2 = gate / (n[12] + 1e-6f);
    float oa[12];
#pragma unroll
    for (int i = 0; i < 12; ++i) oa[i] = n[i] * sc2;
    store_o(ws + WS_OA + orec, oa, h);
  }
  STAMP(1, 4);
}

constexpr int K3_SLOT = 40960, K3_LDS = 3 * K3_SLOT;
__global__ __launch_bounds__(512, 2) void k3_out(const u16* __restrict__ woA, const u16* __restrict__ woB, const char* __restrict__ ws, u16* __restrict__ out) {
  extern __shared__ __attribute__((aligned(16))) char lds[];
  const int tid = threadIdx.x, lane = tid & 63, l16 = lane & 15, g4 = lane >> 4;
  const int wid = __builtin_amdgcn_readfirstlane(tid >> 6);
  const int xcd = blockIdx.x & 7, idx = blockIdx.x >> 3;
  const int rb = (xcd >> 1) * 8 + (idx & 7), cb = (xcd & 1) * 4 + (idx >> 3);
  const int br = wid >> 2, wt = wid & 3;
  const unsigned lds0 = (unsigned)(uintptr_t)lds;
  const char* wsrc = (const char*)(br ? woB : woA) + cb * 160;
  unsigned wo[5];
#pragma unroll
  for (int i = 0; i < 5; ++i) {
    const int ch = (wt * 5 + i) * 64 + lane, rho = ch / 10, c = ch - rho * 10;
    wo[i] = (unsigned)((rho ^ ((rho & 8) >> 1)) * 1280 + c * 16);
  }
  const char* osrc = ws + (br ? WS_OB : WS_OA) + (size_t)(rb * 64 + wt * 16 + l16) * 80;
  bf16x8 of[3][4];
  f32x4 acc[5];
#pragma unroll
  for (int i = 0; i < 5; ++i) acc[i] = f32x4{};
  const lds_cptr lw = (lds_cptr)lds + br * 20480 + (8 * g4 + (l16 >> 2)) * 160 + (l16 & 3) * 8;
  const int t0 = (g4 & 1) * 640, t1 = 640 - t0;
#define K3_STAGE(st) do { \
    const unsigned dst_ = lds0 + ((st) % 3) * K3_SLOT + br * 20480 + wt * 5120; \
    _Pragma("unroll") for (int i = 0; i < 5; ++i) glds16(wsrc + (st) * 128 * 1280, wo[i], dst_ + i * 1024); \
    _Pragma("unroll") for (int i = 0; i < 4; ++i) { \
      const int kc_ = ((st) * 4 + i) * 4 + g4, hp_ = kc_ / 5, c_ = kc_ - hp_ * 5; \
      asm volatile("global_load_dwordx4 %0, %1, off" : "=v"(of[(st) % 3][i]) : "v"(osrc + (size_t)hp_ * S * 80 + c_ * 16) : "memory"); } \
  } while (0)
  STAMP(2, 0);
  K3_STAGE(0); K3_STAGE(1);
#pragma unroll
  for (int st = 0; st < 5; ++st) {
    if (st < 4) asm volatile("s_waitcnt vmcnt(9) lgkmcnt(0)\n\ts_barrier" : "+v"(of[st % 3][0]), "+v"(of[st % 3][1]), "+v"(of[st % 3][2]), "+v"(of[st % 3][3])::"memory");
    else asm volatile("s_waitcnt vmcnt(0) lgkmcnt(0)\n\ts_barrier" : "+v"(of[st % 3][0]), "+v"(of[st % 3][1]), "+v"(of[st % 3][2]), "+v"(of[st % 3][3])::"memory");
    if (st + 2 < 5) K3_STAGE(st + 2);
#pragma unroll
    for (int i = 0; i < 4; ++i)
#pragma unroll
      for (int n = 0; n < 5; ++n) {
        const lds_cptr p = lw + (st % 3) * K3_SLOT + i * 32 * 160 + n * 32;
        acc[n] = MFMA16(cat8(ldtr(p + t0), ldtr(p + t1)), of[st % 3][i], acc[n]);
      }
  }
  asm volatile("s_waitcnt lgkmcnt(0)\n\ts_barrier" ::: "memory");
  STAMP(2, 1);
  const lds_ptr red = (lds_ptr)lds + wt * 5120 + lane * 16;
  if (br) {
#pragma unroll
    for (int n = 0; n < 5; ++n) *(LAS f32x4*)(red + n * 1024) = acc[n];
  }
  asm volatile("s_waitcnt lgkmcnt(0)\n\ts_barrier" ::: "memory");
  if (!br) {
    u16* dst = out + (size_t)(rb * 64 + wt * 16 + l16) * D + cb * 80 + g4 * 4;
#pragma unroll
    for (int n = 0; n < 5; ++n) {
      const f32x4 t = acc[n] + *(LAS const f32x4*)(red + n * 1024);
      u32x2 v = {pk2(t[0], t[1]), pk2(t[2], t[3])};
      *(u32x2*)(dst + n * 16) = v;
    }
  }
  STAMP(2, 2);
}

extern "C" void kernel_launch(void* const* d_in, const int* in_sizes, int n_in, void* d_out, int out_size, void* d_ws, size_t ws_size,
                              hipStream_t stream) {
  const u16* x = (const u16*)d_in[0];
  const u16* wA = (const u16*)d_in[1];
  const u16* wphi = (const u16*)d_in[2];
  const u16* woA = (const u16*)d_in[3];
  const u16* wB = (const u16*)d_in[5];
  const u16* woB = (const u16*)d_in[6];
  const u16* gw = (const u16*)d_in[7];
  (void)hipFuncSetAttribute((const void*)k1_qkv, hipFuncAttributeMaxDynamicSharedMemorySize, K1_LDS);
  (void)hipFuncSetAttribute((const void*)k2_attn, hipFuncAttributeMaxDynamicSharedMemorySize, K2_LDS);
  (void)hipFuncSetAttribute((const void*)k3_out, hipFuncAttributeMaxDynamicSharedMemorySize, K3_LDS);
  k1_qkv<<<256, 512, K1_LDS, stream>>>(x, wA, wB, wphi, gw, (char*)d_ws);
  k2_attn<<<256, 512, K2_LDS, stream>>>((char*)d_ws);
  k3_out<<<256, 512, K3_LDS, stream>>>(woA, woB, (const char*)d_ws, (u16*)d_out);
}
```

```cpp
#include <hip/hip_runtime.h>
#include <stdint.h>

typedef unsigned short u16;
typedef unsigned long long u64;
typedef __attribute__((ext_vector_type(8))) short bf16x8;
typedef __attribute__((ext_vector_type(4))) short s16x4;
typedef __attribute__((ext_vector_type(16))) float f32x16;
typedef __attribute__((ext_vector_type(4))) float f32x4;
typedef __attribute__((ext_vector_type(16))) unsigned u32x16;
typedef __attribute__((ext_vector_type(8))) unsigned u32x8;
typedef __attribute__((ext_vector_type(4))) unsigned u32x4;
typedef __attribute__((ext_vector_type(3))) unsigned u32x3;
typedef __attribute__((ext_vector_type(2))) unsigned u32x2;
typedef __attribute__((ext_vector_type(2))) __bf16 bf16x2_t;
typedef __attribute__((ext_vector_type(2))) float f32x2;
#define LAS __attribute__((address_space(3)))
#define GAS __attribute__((address_space(1)))
typedef LAS const char* lds_cptr;
typedef LAS char* lds_ptr;
#define DI __device__ __forceinline__
#define MFMA32(a, b, c) __builtin_amdgcn_mfma_f32_32x32x16_bf16(a, b, c, 0, 0, 0)
#define MFMA8(a, b, c) __builtin_amdgcn_mfma_f32_32x32x8bf16_1k(a, b, c, 0, 0, 0)
#define MFMA16(a, b, c) __builtin_amdgcn_mfma_f32_16x16x32_bf16(a, b, c, 0, 0, 0)
#define WAIT_BAR(N) asm volatile("s_waitcnt vmcnt(" #N ") lgkmcnt(0)\n\ts_barrier" ::: "memory")
#define LBAR() asm volatile("s_waitcnt lgkmcnt(0)\n\ts_barrier" ::: "memory")
#define BF8(x) __builtin_bit_cast(bf16x8, x)
#define BF4(x) __builtin_bit_cast(s16x4, x)
#define SPIN_CAP (1u << 18)

constexpr int S = 2048, D = 640, H = 32;
constexpr int TQ = 1280;
constexpr int VB = 352;
constexpr int TKV = TQ + 4 * VB;
constexpr int TS_V = H * TQ, TS_C = TS_V + 4 * 640 * 16, TS = TS_C + 512;
constexpr size_t QIMG = (size_t)H * 64 * TQ, KVIMG = (size_t)64 * TS;
constexpr size_t WS_EPOCH = 0;
constexpr size_t WS_FX = 256, WS_FW = WS_FX + 256, WS_FB = WS_FW + 1024, WS_FA = WS_FB + 60 * 256, WS_FKV = WS_FA + 60 * 256, WS_FOA = WS_FKV + 32 * 256,
                 WS_FEND = WS_FOA + 32 * 256;
constexpr size_t WS_QA = 69632, WS_QB = WS_QA + QIMG, WS_KVA = WS_QB + QIMG, WS_KVB = WS_KVA + KVIMG;
constexpr size_t WS_G = WS_KVB + KVIMG;
constexpr size_t WS_PHI = WS_G + 8192;
constexpr size_t WS_SLAB = WS_PHI + 32 * 2048;
constexpr size_t OIMG = (size_t)128 * 20480;
constexpr size_t WS_OA = WS_SLAB + (size_t)32 * 8 * 6144, WS_OB = WS_OA + OIMG;
constexpr size_t WS_W = WS_OB + OIMG;
static_assert(WS_FEND <= WS_QA, "flags");
constexpr float QSCALE = 0.22360679774997896f * 1.4426950408889634f;
constexpr float LOG2E = 1.4426950408889634f;
constexpr u64 TAG_STEP = 0x9E3779B97F4A7C15ull;
constexpr int STG = 8 * TKV, L_XS = 49152, L_S0 = 76800, L_SY = L_S0 + 4 * STG, LDS_BYTES = L_SY + 1024;
constexpr int kv_slot(int s) { return L_S0 - (s < 3 ? s : s - 6) * STG; }
constexpr int L_PHIK = 0, L_KVB = 24576, L_W0 = 0, L_W1 = L_S0 + STG, L_Q = L_XS + 6 * 16384, L_MRG0 = L_W1 + 51200, L_MRG1 = kv_slot(6), L_APART = kv_slot(7), L_G = L_SY - 1024;
constexpr int SY_LAND = L_SY, SY_PROG = L_SY + 64, SY_LBAR = L_SY + 96, SY_WLAND = L_SY + 100, SY_GATE = L_SY + 104, SY_LPROG = L_SY + 112, SY_MID = SY_PROG + 64, SY_TAG = L_SY + 160;
static_assert(L_W1 - L_W0 == 98304 && SY_LBAR - L_SY == 96 && SY_WLAND - SY_LBAR == 4 && SY_GATE - SY_LBAR == 8, "service tail");
static_assert(L_KVB + 3168 <= kv_slot(2) && L_W0 + 51200 <= kv_slot(7) && L_Q + 12288 + 2048 <= L_SY && L_MRG0 + 4 * 3072 <= L_G && LDS_BYTES <= 163840, "lds map");
#define OTA 0x3210876459ull
#define XT1 0x20111B11111ull
#define XT2 0x00E11111CFull
#define LSPIN (1u << 18)

DI unsigned pk2(float lo, float hi) {
  f32x2 v = {lo, hi};
  return __builtin_bit_cast(unsigned, __builtin_convertvector(v, bf16x2_t));
}
DI float b2f(u16 v) { return __uint_as_float(((unsigned)v) << 16); }
DI int uni(int v) { return __builtin_amdgcn_readfirstlane(v); }
DI const char* uni(const char* p) {
  const u64 v = (u64)p;
  return (const char*)(((u64)(unsigned)uni((int)(v >> 32)) << 32) | (unsigned)uni((int)v));
}
DI void glds16(const void* base, unsigned voff, unsigned lds_dst) {
  asm volatile("s_mov_b32 m0, %2\n\ts_nop 0\n\tglobal_load_lds_dwordx4 %0, %1" ::"v"(voff), "s"(base), "s"(lds_dst) : "memory");
}
DI void glds16c(const void* base, unsigned voff, unsigned lds_dst) {
  asm volatile("s_mov_b32 m0, %2\n\ts_nop 0\n\tglobal_load_lds_dwordx4 %0, %1 sc1" ::"v"(voff), "s"(base), "s"(lds_dst) : "memory");
}
DI void glds64c(const void* base, unsigned voff, unsigned lds_dst) {
  asm volatile("s_mov_b32 m0, %2\n\ts_nop 0\n\tglobal_load_lds_dwordx4 %0, %1 sc1\n\tglobal_load_lds_dwordx4 %0, %1 offset:1024 sc1\n\t"
               "global_load_lds_dwordx4 %0, %1 offset:2048 sc1\n\tglobal_load_lds_dwordx4 %0, %1 offset:3072 sc1" ::"v"(voff), "s"(base), "s"(lds_dst) : "memory");
}
DI void touch(const void* base, unsigned voff, unsigned lds_dst) {
  asm volatile("s_mov_b32 m0, %2\n\ts_nop 0\n\tglobal_load_lds_dword %0, %1" ::"v"(voff), "s"(base), "s"(lds_dst) : "memory");
}
#define VMWAIT(N) asm volatile("s_waitcnt vmcnt(" #N ")" ::: "memory")
template <int SLEEP = 1>
DI void lds_wait(unsigned a, unsigned v) {
#pragma nounroll
  for (unsigned i = 0; i < LSPIN; ++i) {
    unsigned t;
    asm volatile("ds_read_b32 %0, %1\n\ts_waitcnt lgkmcnt(0)" : "=v"(t) : "v"(a) : "memory");
    if (__all(t >= v)) break;
    __builtin_amdgcn_s_sleep(SLEEP);
  }
}
DI void lds_post(unsigned a, unsigned v, bool on) {
  asm volatile("s_waitcnt lgkmcnt(0)" ::: "memory");
  if (on) asm volatile("ds_write_b32 %0, %1" ::"v"(a), "v"(v) : "memory");
}
DI void lds_add(unsigned a, bool on) {
  asm volatile("s_waitcnt lgkmcnt(0)" ::: "memory");
  if (on) asm volatile("ds_add_u32 %0, %1" ::"v"(a), "v"(1u) : "memory");
}
DI void st16(void* p, u32x4 v) { asm volatile("global_store_dwordx4 %0, %1, off sc1\n\ts_nop 1" ::"v"(p), "v"(v) : "memory"); }
DI void st8(void* p, u32x2 v) { asm volatile("global_store_dwordx2 %0, %1, off sc1" ::"v"(p), "v"(v) : "memory"); }
DI void st4(void* p, unsigned v) { asm volatile("global_store_dword %0, %1, off sc1" ::"v"(p), "v"(v) : "memory"); }
DI void set_flag(char* p, u64 tag) { __hip_atomic_store((GAS u64*)p, tag, __ATOMIC_RELAXED, __HIP_MEMORY_SCOPE_AGENT); }
DI void poll_flags(const char* p, u64 tag) {
#pragma nounroll
  for (unsigned spins = 0; spins < SPIN_CAP; ++spins) {
    const u64 v = __hip_atomic_load((GAS const u64*)p, __ATOMIC_RELAXED, __HIP_MEMORY_SCOPE_AGENT);
    if (__all(v == tag)) break;
    __builtin_amdgcn_s_sleep(1);
  }
}
DI void poll_pairs(const char* base, unsigned off, u64 tag) {
#pragma nounroll
  for (unsigned spins = 0; spins < SPIN_CAP; ++spins) {
    u32x4 v;
    asm volatile("global_load_dwordx4 %0, %1, %2 sc1\n\ts_waitcnt vmcnt(0)" : "=v"(v) : "v"(off), "s"(base) : "memory");
    if (__all(v[0] == (unsigned)tag && v[1] == (unsigned)(tag >> 32) && v[2] == (unsigned)tag && v[3] == (unsigned)(tag >> 32))) break;
    __builtin_amdgcn_s_sleep(1);
  }
}
DI s16x4 ldtr(lds_cptr p) {
  typedef short v4i16_t __attribute__((ext_vector_type(4)));
  return __builtin_bit_cast(s16x4, __builtin_amdgcn_ds_read_tr16_b64_v4i16((LAS v4i16_t*)p));
}
DI bf16x8 cat8(s16x4 a, s16x4 b) { return __builtin_shufflevector(a, b, 0, 1, 2, 3, 4, 5, 6, 7); }
DI float max_halves(float x) {
  auto rr = __builtin_amdgcn_permlane32_swap(__float_as_uint(x), __float_as_uint(x), false, false);
  return __builtin_fmaxf(__uint_as_float(rr[0]), __uint_as_float(rr[1]));
}
DI float sum_halves(float x) {
  auto rr = __builtin_amdgcn_permlane32_swap(__float_as_uint(x), __float_as_uint(x), false, false);
  return __uint_as_float(rr[0]) + __uint_as_float(rr[1]);
}
DI s16x4 ld8c(const char* p) { return __builtin_bit_cast(s16x4, __hip_atomic_load((GAS const u64*)p, __ATOMIC_RELAXED, __HIP_MEMORY_SCOPE_AGENT)); }
DI float ld4c(const char* p) { return __uint_as_float(__hip_atomic_load((GAS const unsigned*)p, __ATOMIC_RELAXED, __HIP_MEMORY_SCOPE_AGENT)); }
DI bf16x8 gfrag1(const char* tile, int p, int h) { return cat8(ld8c(tile + h * 512 + p), ld8c(tile + h * 512 + 256 + p)); }
DI s16x4 gfrag2(const char* tile, int p) { return ld8c(tile + 1024 + p); }

DI float vmax(float a, float b) { float d; asm("v_max_f32 %0, %1, %2" : "=v"(d) : "v"(a), "v"(b)); return d; }
DI float vmax3(float a, float b, float c) { float d; asm("v_max3_f32 %0, %1, %2, %3" : "=v"(d) : "v"(a), "v"(b), "v"(c)); return d; }
DI float vmin3(float a, float b, float c) { float d; asm("v_min3_f32 %0, %1, %2, %3" : "=v"(d) : "v"(a), "v"(b), "v"(c)); return d; }
DI float vmaxn(float a, float b) { float d; asm("v_max_f32_e64 %0, -%1, -%2" : "=v"(d) : "v"(a), "v"(b)); return d; }
DI unsigned umax3(unsigned a, unsigned b, unsigned c) { return __builtin_elementwise_max(__builtin_elementwise_max(a, b), c); }
DI f32x2 swap32(float a, float b) {
  auto rr = __builtin_amdgcn_permlane32_swap(__float_as_uint(a), __float_as_uint(b), false, false);
  return f32x2{__uint_as_float(rr[0]), __uint_as_float(rr[1])};
}
template <bool NORM>
DI void featmap(const bf16x8 f1, const s16x4 f2, const bf16x8 (&x1)[2], const s16x4 (&x2)[2], u32x4 (&pk)[2][3]) {
  f32x16 y[2] = {MFMA32(f1, x1[0], f32x16{}), MFMA32(f1, x1[1], f32x16{})};
  y[0] = MFMA8(f2, x2[0], y[0]);
  y[1] = MFMA8(f2, x2[1], y[1]);
  asm volatile("s_nop 11" : "+v"(y[0]), "+v"(y[1]));
  float mx[2], nm[2], ep[2][10], em[2][10];
  f32x2 t[2];
#pragma unroll
  for (int k = 0; k < 2; ++k) { mx[k] = vmax3(y[k][0], y[k][1], y[k][2]); nm[k] = vmin3(y[k][0], y[k][1], y[k][2]); }
#pragma unroll
  for (int i = 3; i < 9; i += 2)
#pragma unroll
    for (int k = 0; k < 2; ++k) { mx[k] = vmax3(mx[k], y[k][i], y[k][i + 1]); nm[k] = vmin3(nm[k], y[k][i], y[k][i + 1]); }
#pragma unroll
  for (int k = 0; k < 2; ++k) t[k] = swap32(vmax(mx[k], y[k][9]), vmaxn(nm[k], y[k][9]));
#pragma unroll
  for (int k = 0; k < 2; ++k) { const float tm = vmax(t[k][0], t[k][1]) * -LOG2E; t[k] = swap32(tm, tm); }
#pragma unroll
  for (int i = 0; i < 10; ++i)
#pragma unroll
    for (int k = 0; k < 2; ++k) {
      ep[k][i] = __builtin_amdgcn_exp2f(__builtin_fmaf(y[k][i], LOG2E, t[k][0]));
      em[k][i] = __builtin_amdgcn_exp2f(__builtin_fmaf(y[k][i], -LOG2E, t[k][1]));
    }
#pragma unroll
  for (int k = 0; k < 2; ++k) {
    if (NORM) {
      float sp = ep[k][0], sm = em[k][0];
#pragma unroll
      for (int i = 1; i < 10; ++i) { sp += ep[k][i]; sm += em[k][i]; }
      f32x2 u = swap32(sp, sm);
      const float rs = __builtin_amdgcn_rcpf(u[0] + u[1]);
      u = swap32(rs, rs);
#pragma unroll
      for (int i = 0; i < 10; ++i) { ep[k][i] *= u[0]; em[k][i] *= u[1]; }
    }
#pragma unroll
    for (int i = 0; i < 4; ++i) { pk[k][0][i] = pk2(ep[k][2 * i], ep[k][2 * i + 1]); pk[k][2][i] = pk2(em[k][2 * i], em[k][2 * i + 1]); }
    pk[k][1] = u32x4{pk2(ep[k][8], ep[k][9]), 0u, pk2(em[k][8], em[k][9]), 0u};
  }
}
DI float rowmax16(const f32x16& s) {
  float a = __builtin_fmaxf(__builtin_fmaxf(s[0], s[1]), s[2]);
#pragma unroll
  for (int i = 3; i < 15; i += 2) a = __builtin_fmaxf(__builtin_fmaxf(a, s[i]), s[i + 1]);
  a = __builtin_fmaxf(a, s[15]);
  return max_halves(a);
}
DI void store_o(char* img, int tile, int head, u32x4 v, u32x2 w, int r, int h) {
  const unsigned tb = (tile * 2 + (r >> 4)) * 20480;
  asm volatile("global_store_dwordx4 %0, %1, %2 sc1" ::"v"(tb + (head * 2 + h) * 256 + (r & 15) * 16), "v"(v), "s"(img) : "memory");
  if (h == 0) asm volatile("global_store_dwordx2 %0, %1, %2 sc1" ::"v"(tb + 16384 + head * 128 + (r & 15) * 8), "v"(w), "s"(img) : "memory");
  asm volatile("s_nop 1");
}
#define GEMM_OPS "+{v[0:15]}"(at), "={v[64:79]}"(o0), "={v[80:95]}"(o1), "={v[100:101]}"(z0), "={v[112:115]}"(z1), "+{v[160:163]}"(ad), [xt] "+s"(xt), [sc] "+s"(sc), \
        [sl] "=&s"(sl), [st] "=&s"(st), [cnt] "=&s"(cnt), [tmp] "=&s"(tmp) \
      : "{v[164:165]}"(so), [xt2] "s"(XT2), [sb0] "s"(sb), [sb1] "s"(sb + ts), [sa0] "s"(sa), [sa1] "s"(sa + ts), [fl] "s"(fb), [ta] "s"(lds0 + SY_TAG), \
        [tl] "s"(tl), [ty] "s"(type) \
      : "v16", "v17", "v18", "v19", "v20", "v21", "v22", "v23", "v24", "v25", "v26", "v27", "v28", "v29", "v30", "v31", "v32", "v33", "v34", "v35", "v36", "v37", "v38", "v39", "v40", "v41", "v42", "v43", "v44", "v45", "v46", "v47", "v48", "v49", "v50", "v51", "v52", "v53", "v54", "v55", "v56", "v57", "v58", "v59", "v60", "v61", "v62", "v63", "v96", "v97", "v98", "v99", "v102", "v103", "v104", "v105", "v106", "v107", "v108", "v109", "v110", "v111", "v116", "v117", "v118", "v119", "v120", "v121", "v122", "v123", "v124", "v125", "v126", "v127", "v128", "v129", "v130", "v131", "v132", "v133", "v134", "v135", "v136", "v137", "v138", "v139", "v140", "v141", "v142", "v143", "v144", "v145", "v146", "v147", "v148", "v149", "v150", "v151", "v152", "v153", "v154", "v155", "v156", "v157", "v158", "v159", "s96", "s97", "s98", "s99", "vcc", "scc", "memory"
#define LOAD_OPS : "={v[56:57]}"(tag), "={v[60:63]}"(ka), "={v[64:65]}"(kb), "+{v[44:47]}"(o3), "+{v59}"(stampv), [ot] "+s"(ot), [xt] "+s"(xt), [wb] "+s"(wb), [done] "=s"(done) \
      : "{v[32:39]}"(o1), "{v[40:43]}"(o2), "{v58}"(lds0 + SY_TAG), [ws] "s"(ws), [otA] "s"(OTA), [xtA] "s"(0x3543FFFFFFull), [wa] "s"((const char*)wA + col * 256), \
        [xb] "s"((const char*)x + (size_t)row * 128 * 1280), [rot] "s"(rot), [ld] "s"(lds0 + lw * 4096), [kd] "s"(lds0 + lw * 5376), [qd] "s"(lds0 + L_Q + lw * 3072), [kvs] "s"(kvg), \
        [kv1] "s"(kvg + 8 * TS), [hq] "s"(head * TQ), [hv] "s"(TS_V + head * 320), [hc] "s"(TS_C - 320 + (head >> 1) * 32), [tst] "s"(TS), [tlo] "s"((unsigned)TAG_STEP), [thi] "s"((unsigned)(TAG_STEP >> 32)) \
      : "v48", "v49", "v50", "v51", "v52", "v53", "v54", "v55", "s76", "s77", "s78", "s79", "s80", "s81", "s82", "s83", "s84", "s85", "s86", "s87", "s88", "s89", "s90", "s91", "s92", "s93", "s94", "s95", "vcc", "scc", "memory"

DI int lane_id() { int l; asm volatile("v_mbcnt_lo_u32_b32 %0, -1, 0\n\tv_mbcnt_hi_u32_b32 %0, -1, %0" : "=v"(l)); __builtin_assume(l >= 0 && l < 64); return l; }
#define LANES [[maybe_unused]] const int lane = lane_id(), tid = wid * 64 + lane, r = lane & 31, h = lane >> 5, l16 = lane & 15, g4 = lane >> 4, vslot = r < 20 ? r : ((r & 3) == 0 && r >= 24 ? 20 : 21), \
                             posq = (r & 15) * 16 + (r >> 4) * 8, posk = (r >> 4) * 128 + (r & 7) * 16 + ((r >> 3) & 1) * 8

__global__ __launch_bounds__(768, 3) void fused(const u16* __restrict__ x, const u16* __restrict__ wA, const u16* __restrict__ wB, char* __restrict__ ws,
                                                 u16* __restrict__ out, const u16* __restrict__ woA, const u16* __restrict__ woB,
                                                 const u16* __restrict__ wphi, const u16* __restrict__ gw) {
  extern __shared__ __attribute__((aligned(16))) char lds[];
  asm volatile(
      "v_readfirstlane_b32 s82, %[t]\n"
      "s_cmp_ge_u32 %[b], 240\n"
      "s_cbranch_scc1 Lq%=\n"
      "s_sub_u32 s82, s82, 512\n"
      "s_cbranch_scc1 Lq%=\n"
      "s_setprio 3\n"
      "s_load_dwordx2 s[78:79], %[ka], 0x10\n"
      "s_load_dwordx2 s[76:77], %[ka], 0x0\n"
      "v_bfe_u32 v40, %[t], 4, 2\n"
      "v_bfe_u32 v41, %[t], 3, 5\n"
      "v_and_b32 v42, 15, %[t]\n"
      "v_bfe_u32 v43, %[t], 4, 4\n"
      "s_and_b32 s83, %[b], 3\n"
      "s_mul_i32 s84, s83, 30\n"
      "s_lshr_b32 s85, %[b], 3\n"
      "s_add_u32 s84, s84, s85\n"
      "v_lshrrev_b32 v44, 2, v43\n"
      "v_lshlrev_b32 v45, 5, v40\n"
      "v_lshl_add_u32 v44, v44, 4, v40\n"
      "v_lshlrev_b32 v42, 4, v42\n"
      "s_and_b32 s85, s84, 7\n"
      "s_lshl_b32 s85, s85, 2\n"
      "s_lshr_b32 s83, 0x87653210, s85\n"
      "s_and_b32 s83, s83, 15\n"
      "s_and_b32 s85, %[b], 7\n"
      "s_lshl_b32 s85, s85, 2\n"
      "s_lshr_b32 s85, 0x86317520, s85\n"
      "s_and_b32 s85, s85, 15\n"
      "s_add_u32 s83, s83, s85\n"
      "s_cmp_ge_u32 s83, 10\n"
      "s_cselect_b32 s85, 10, 0\n"
      "s_sub_u32 s83, s83, s85\n"
      "s_bfe_u32 s85, %[b], 0x10002\n"
      "v_mul_u32_u24 v44, 0xf00, v44\n"
      "v_xor_b32 v45, v45, v42\n"
      "v_lshrrev_b32 v46, 3, v41\n"
      "v_and_b32 v41, 7, v41\n"
      "s_lshr_b32 s86, s84, 3\n"
      "s_lshl_b32 s86, s86, 8\n"
      "s_mul_i32 s87, s83, 245760\n"
      "s_add_u32 s86, s86, s87\n"
      "v_add_u32 v36, v44, v45\n"
      "v_xor_b32 v45, 0x80, v45\n"
      "v_lshl_add_u32 v41, v46, 5, v41\n"
      "v_and_b32 v46, 7, %[t]\n"
      "s_and_b32 s84, s84, 7\n"
      "s_lshl3_add_u32 s84, s85, s84\n"
      "s_mul_i32 s84, s84, 1280\n"
      "s_add_u32 s84, s84, s83\n"
      "v_add_u32 v37, 0x3800, v36\n"
      "v_add_u32 v44, v44, v45\n"
      "v_mul_u32_u24 v41, 0x500, v41\n"
      "v_xor_b32 v46, v46, v40\n"
      "s_lshl_b32 s84, s84, 7\n"
      "s_lshl_b32 s82, s82, 6\n"
      "s_add_u32 s82, s82, %[l0]\n"
      "s_mov_b32 m0, s82\n"
      "v_add_u32 v38, 0x7000, v44\n"
      "v_add_u32 v39, 0xa800, v44\n"
      "v_lshl_add_u32 v32, v46, 4, v41\n"
      "v_xor_b32 v46, 4, v46\n"
      "s_cmp_eq_u32 s83, 9\n"
      "s_cselect_b32 s87, -9, 1\n"
      "s_mul_i32 s85, s87, 245760\n"
      "s_lshl_b32 s87, s87, 7\n"
      "s_waitcnt lgkmcnt(0)\n"
      "s_add_u32 s78, s78, s86\n"
      "s_addc_u32 s79, s79, 0\n"
      "global_load_lds_dwordx4 v36, s[78:79] offset:0\n"
      "v_lshl_add_u32 v41, v46, 4, v41\n"
      "global_load_lds_dwordx4 v37, s[78:79] offset:1024\n"
      "s_add_u32 s76, s76, s84\n"
      "v_add_u32 v34, 0x4800, v32\n"
      "global_load_lds_dwordx4 v38, s[78:79] offset:2048\n"
      "s_addc_u32 s77, s77, 0\n"
      "v_add_u32 v33, 0x2400, v41\n"
      "global_load_lds_dwordx4 v39, s[78:79] offset:3072\n"
      "v_add_u32 v35, 0x6c00, v41\n"
      "s_add_u32 m0, s82, 49152\n"
      "s_ashr_i32 s83, s85, 31\n"
      "global_load_lds_dwordx4 v32, s[76:77] offset:0\n"
      "s_add_u32 s80, s78, s85\n"
      "s_addc_u32 s81, s79, s83\n"
      "global_load_lds_dwordx4 v33, s[76:77] offset:1024\n"
      "global_load_lds_dwordx4 v34, s[76:77] offset:2048\n"
      "global_load_lds_dwordx4 v35, s[76:77] offset:3072\n"
      "s_add_u32 m0, s82, 0x4000\n"
      "s_nop 0\n"
      "global_load_lds_dwordx4 v36, s[80:81] offset:0\n"
      "global_load_lds_dwordx4 v37, s[80:81] offset:1024\n"
      "global_load_lds_dwordx4 v38, s[80:81] offset:2048\n"
      "global_load_lds_dwordx4 v39, s[80:81] offset:3072\n"
      "s_add_u32 m0, s82, 65536\n"
      "s_ashr_i32 s83, s87, 31\n"
      "s_add_u32 s76, s76, s87\n"
      "s_addc_u32 s77, s77, s83\n"
      "global_load_lds_dwordx4 v32, s[76:77] offset:0\n"
      "global_load_lds_dwordx4 v33, s[76:77] offset:1024\n"
      "global_load_lds_dwordx4 v34, s[76:77] offset:2048\n"
      "global_load_lds_dwordx4 v35, s[76:77] offset:3072\n"
      "Lq%=:\n"
      :: [t] "v"(threadIdx.x), [b] "s"(blockIdx.x), [ka] "s"(__builtin_amdgcn_kernarg_segment_ptr()), [l0] "s"((unsigned)(uintptr_t)lds)
      : "v32", "v33", "v34", "v35", "v36", "v37", "v38", "v39", "v40", "v41", "v42", "v43", "v44", "v45", "v46", "s76", "s77", "s78", "s79", "s80", "s81", "s82", "s83", "s84", "s85", "s86", "s87", "scc", "memory");
  const int wid = __builtin_amdgcn_readfirstlane(threadIdx.x >> 6);
  const int xcd = blockIdx.x & 7, idx = blockIdx.x >> 3;
  const unsigned lds0 = (unsigned)(uintptr_t)lds;
  const int rem = (xcd & 3) * 30 + idx, col = rem >> 3, row = (xcd >> 2) * 8 + (rem & 7);
  const int rot0 = ((0x87653210u >> ((rem & 7) * 4)) & 15) + (((col & 3) * 0) >> 1) + ((0x86317520u >> (xcd * 4)) & 15);
  const int rot = rot0 - (rot0 >= 20 ? 20 : rot0 >= 10 ? 10 : 0);
#define CPF_WHO (wid < 8 && idx >= 30)
  if (CPF_WHO) {
    u64 pc;
    int len;
    asm volatile("s_getpc_b64 %0\nLfused_beg:\n\ts_mov_b32 %1, Lfused_end-Lfused_beg" : "=s"(pc), "=s"(len));
    if (threadIdx.x * 128 < len) touch((const void*)pc, threadIdx.x * 128, lds0 + L_Q + 12288 + wid * 256);
  }
  const int head = xcd * 4 + (idx & 3), qb = idx >> 2;
  const int rb3 = xcd * 4 + (idx & 3), cb3 = idx >> 2;
  const char* kvg = ws + WS_KVB;
  if (wid >= 8) {
    const int lw = wid - 8;
    LANES;
    u64 tag;
    u32x4 ka;
    u32x2 kb;
    auto kv_copy = [&](const char* src, int slot) {
      asm volatile("s_mov_b32 m0, %7\n\ts_nop 0\n\tglobal_load_lds_dwordx4 %0, %6 sc1\n\tglobal_load_lds_dwordx4 %1, %6 offset:1024 sc1\n\t"
                   "global_load_lds_dwordx4 %2, %6 offset:2048 sc1\n\tglobal_load_lds_dwordx4 %3, %6 offset:3072 sc1\n\ts_add_u32 m0, %7, 4096\n\ts_nop 0\n\t"
                   "global_load_lds_dwordx4 %4, %6 sc1\n\ts_mov_b64 exec, 0xffff\n\tglobal_load_lds_dwordx4 %5, %6 offset:1024 sc1\n\ts_mov_b64 exec, -1"
                   ::"v"(ka[0]), "v"(ka[1]), "v"(ka[2]), "v"(ka[3]), "v"(kb[0]), "v"(kb[1]), "s"(src), "s"(lds0 + slot + lw * 5376) : "scc", "memory");
    };
    auto kv_dma = [&](int st) { kv_copy(kvg + st * 8 * TS, kv_slot(st)); };
    unsigned qo[3];
#pragma unroll
    for (int i = 0; i < 3; ++i)
      qo[i] = (unsigned)WS_QB + (head * 64 + qb * 8) * TQ + lw * 3072 - (lw == 3 ? i * 1024 : 0) + lane * 16;
    if (lw == 3 && lane < 10) *(LAS u32x4*)((lds_ptr)lds + L_SY + lane * 16) = u32x4{lane == 2 ? 4u : 0u, 0u, 0u, 0u};
    const unsigned fb1 = (unsigned)WS_FB + ((640 * (1 + (g4 >> 1)) + 20 * head + (g4 & 1) * 19) >> 5) * 256 + l16 * 16;
    const unsigned fb2 = (lane & 4) == 0 ? (unsigned)WS_FB + ((20 * head + (lane & 1) * 19) >> 5) * 256 + (qb * 2 + ((lane >> 1) & 1)) * 16
                                         : (unsigned)WS_FX + (head >> 1) * 16;
    int done = 0;
    if (idx < 30) {
      u32x8 o1;
#pragma unroll
      for (int i = 0; i < 4; ++i) {
        o1[i] = (unsigned)((lw * 32 + i * 8 + (lane >> 3)) * 1280 + (((lane & 7) ^ ((i & 1) * 4 + g4)) * 16) - i * 1024);
        o1[4 + i] = (unsigned)((lw * 16 + i * 4 + g4) * 3840 + ((l16 ^ ((g4 | ((i & 2) * 2)) << 1)) * 16) - i * 1024);
      }
      const u32x4 o2 = {fb1, fb2, qo[0], qo[1]};
      u32x4 o3 = {qo[2], (unsigned)(lw * 336 + lane), 0u, (unsigned)WS_EPOCH};
      u64 ot = 0x98765432ull, xt = 0x32105432ull;
      const char* wb = (const char*)wB + col * 256;
      unsigned stampv = 0;
      asm volatile(
      "global_load_dwordx2 v[56:57], v47, %[ws] sc1\n"
      "s_mov_b32 s78, 0\n"
      "s_mov_b32 s79, 8\n"
      "s_mov_b32 s80, 0\n"
      "s_mov_b32 s81, 0\n"
      "s_mov_b32 s82, 0x8000\n"
      "s_mov_b32 s76, 1\n"
      "s_cmp_eq_u32 s76, 9\n"
      "s_cselect_b64 %[ot], %[otA], %[ot]\n"
      "s_cselect_b64 %[xt], %[xtA], %[xt]\n"
      "s_cselect_b64 %[wb], %[wa], %[wb]\n"
      "s_bfe_u64 s[92:93], %[ot], 0x40000\n"
      "s_add_u32 s84, s92, %[rot]\n"
      "s_cmp_ge_u32 s84, 10\n"
      "s_cselect_b32 s83, 10, 0\n"
      "s_sub_u32 s84, s84, s83\n"
      "s_lshr_b64 %[ot], %[ot], 4\n"
      "s_bfe_u64 s[92:93], %[xt], 0x40000\n"
      "s_lshr_b64 %[xt], %[xt], 4\n"
      "s_mul_i32 s83, s84, 245760\n"
      "s_mov_b64 s[88:89], %[wb]\n"
      "s_add_u32 s88, s88, s83\n"
      "s_addc_u32 s89, s89, 0\n"
      "s_add_u32 s85, %[ld], s82\n"
      "s_add_u32 s82, s82, 0x4000\n"
      "s_cmp_eq_u32 s82, 0xc000\n"
      "s_cselect_b32 s82, 0, s82\n"
      "s_cmp_eq_u32 s92, 15\n"
      "s_cselect_b32 s87, 4, 8\n"
      "s_cmp_ge_u32 s76, 19\n"
      "s_cselect_b32 s87, 0, s87\n"
      "s_lshl_b32 s83, s84, 7\n"
      "s_mov_b64 s[90:91], %[xb]\n"
      "s_add_u32 s90, s90, s83\n"
      "s_addc_u32 s91, s91, 0\n"
      "s_lshl_b32 s83, s92, 14\n"
      "s_add_u32 s83, s83, 49152\n"
      "s_add_u32 s86, %[ld], s83\n"
      "s_mov_b32 s76, 2\n"
      "s_mov_b32 s77, 8\n"
      "Ll%=:\n"
      "s_cmp_eq_u32 s79, 8\n"
      "s_cbranch_scc0 Lx%=\n"
      "s_waitcnt vmcnt(8)\n"
      "Ld%=:\n"
      "s_barrier\n"
      "s_cmp_ge_u32 s76, 19\n"
      "s_cbranch_scc1 Lf%=\n"
      "Lb%=:\n"
      "s_cmp_eq_u32 s77, 0\n"
      "s_cbranch_scc1 Ls%=\n"
      "s_mov_b32 m0, s85\n"
      "s_nop 0\n"
      "global_load_lds_dwordx4 v36, s[88:89] offset:0\n"
      "s_cmp_eq_u32 s76, 9\n"
      "s_cselect_b64 %[ot], %[otA], %[ot]\n"
      "s_cselect_b64 %[xt], %[xtA], %[xt]\n"
      "global_load_lds_dwordx4 v37, s[88:89] offset:1024\n"
      "s_cselect_b64 %[wb], %[wa], %[wb]\n"
      "s_bfe_u64 s[92:93], %[ot], 0x40000\n"
      "s_add_u32 s84, s92, %[rot]\n"
      "global_load_lds_dwordx4 v38, s[88:89] offset:2048\n"
      "s_cmp_ge_u32 s84, 10\n"
      "s_cselect_b32 s83, 10, 0\n"
      "s_sub_u32 s84, s84, s83\n"
      "global_load_lds_dwordx4 v39, s[88:89] offset:3072\n"
      "s_lshr_b64 %[ot], %[ot], 4\n"
      "s_bfe_u64 s[92:93], %[xt], 0x40000\n"
      "s_lshr_b64 %[xt], %[xt], 4\n"
      "s_cmp_eq_u32 s77, 4\n"
      "s_cbranch_scc1 Lh%=\n"
      "s_mov_b32 m0, s86\n"
      "s_nop 0\n"
      "global_load_lds_dwordx4 v32, s[90:91] offset:0\n"
      "s_mul_i32 s83, s84, 245760\n"
      "s_mov_b64 s[88:89], %[wb]\n"
      "s_add_u32 s88, s88, s83\n"
      "global_load_lds_dwordx4 v33, s[90:91] offset:1024\n"
      "s_addc_u32 s89, s89, 0\n"
      "s_add_u32 s85, %[ld], s82\n"
      "s_add_u32 s82, s82, 0x4000\n"
      "global_load_lds_dwordx4 v34, s[90:91] offset:2048\n"
      "s_cmp_eq_u32 s82, 0xc000\n"
      "s_cselect_b32 s82, 0, s82\n"
      "s_cmp_eq_u32 s92, 15\n"
      "global_load_lds_dwordx4 v35, s[90:91] offset:3072\n"
      "s_cselect_b32 s87, 4, 8\n"
      "s_cmp_ge_u32 s76, 19\n"
      "s_cselect_b32 s87, 0, s87\n"
      "Li%=:\n"
      "s_lshl_b32 s83, s84, 7\n"
      "s_mov_b64 s[90:91], %[xb]\n"
      "s_add_u32 s90, s90, s83\n"
      "s_addc_u32 s91, s91, 0\n"
      "s_lshl_b32 s83, s92, 14\n"
      "s_add_u32 s83, s83, 49152\n"
      "s_add_u32 s86, %[ld], s83\n"
      "Ls%=:\n"
      "s_add_u32 s79, s77, s78\n"
      "s_mov_b32 s77, s87\n"
      "s_cmp_lg_u32 s80, 0\n"
      "s_cbranch_scc1 Lk%=\n"
      "Ln%=:\n"
      "s_add_u32 s76, s76, 1\n"
      "s_cmp_lg_u32 s76, 4\n"
      "s_cbranch_scc0 Lg%=\n"
      "s_sub_u32 s83, s76, 12\n"
      "s_cmp_lt_u32 s83, 6\n"
      "s_cbranch_scc1 Lo%=\n"
      "s_cmp_lt_u32 s76, 22\n"
      "s_cbranch_scc1 Ll%=\n"
      "s_branch Le%=\n"
      "Lh%=:\n"
      "s_mul_i32 s83, s84, 245760\n"
      "s_mov_b64 s[88:89], %[wb]\n"
      "s_add_u32 s88, s88, s83\n"
      "s_addc_u32 s89, s89, 0\n"
      "s_add_u32 s85, %[ld], s82\n"
      "s_add_u32 s82, s82, 0x4000\n"
      "s_cmp_eq_u32 s82, 0xc000\n"
      "s_cselect_b32 s82, 0, s82\n"
      "s_cmp_eq_u32 s92, 15\n"
      "s_cselect_b32 s87, 4, 8\n"
      "s_cmp_ge_u32 s76, 19\n"
      "s_cselect_b32 s87, 0, s87\n"
      "s_branch Li%=\n"
      "Lg%=:\n"
      "s_waitcnt vmcnt(16)\n"
      "v_add_co_u32 v56, vcc, %[tlo], v56\n"
      "v_mov_b32 v48, %[thi]\n"
      "s_nop 0\n"
      "v_addc_co_u32 v57, vcc, v48, v57, vcc\n"
      "s_mov_b64 exec, 1\n"
      "ds_write_b64 v58, v[56:57]\n"
      "s_mov_b64 exec, -1\n"
      "s_waitcnt lgkmcnt(0)\n"
      "s_branch Ll%=\n"
      "Lo%=:\n"
      "v_mul_u32_u24 v48, 3121, v45\n"
      "v_lshrrev_b32 v48, 19, v48\n"
      "s_and_b32 s83, s83, 3\n"
      "v_mul_u32_u24 v49, 168, v48\n"
      "s_lshl_b32 s83, s83, 10\n"
      "v_sub_u32 v49, v45, v49\n"
      "v_subrev_u32 v50, 80, v49\n"
      "v_add_u32 v45, 64, v45\n"
      "v_mul_u32_u24 v51, 745, v50\n"
      "v_lshrrev_b32 v51, 14, v51\n"
      "v_mul_u32_u24 v52, 22, v51\n"
      "v_sub_u32 v50, v50, v52\n"
      "v_lshrrev_b32 v52, 1, v50\n"
      "v_xor_b32 v52, v52, v50\n"
      "v_bfe_u32 v52, v52, 2, 1\n"
      "v_mul_u32_u24 v52, 12, v52\n"
      "v_xor_b32 v52, v52, v50\n"
      "v_lshl_add_u32 v52, v52, 4, %[hv]\n"
      "v_mul_u32_u24 v51, 10240, v51\n"
      "v_add_u32 v52, v52, v51\n"
      "v_lshl_add_u32 v53, v49, 4, %[hq]\n"
      "v_cmp_gt_u32 vcc, 80, v49\n"
      "v_cmp_lt_u32 s[92:93], 19, v50\n"
      "v_cndmask_b32 v52, v52, v53, vcc\n"
      "s_andn2_b64 vcc, s[92:93], vcc\n"
      "v_mad_u32_u24 v52, v48, %[tst], v52\n"
      "v_lshl_add_u32 v53, v50, 4, %[hc]\n"
      "v_mov_b32 v60, v61\n"
      "v_mov_b32 v61, v62\n"
      "v_cndmask_b32 v52, v52, v53, vcc\n"
      "v_mov_b32 v62, v63\n"
      "v_mov_b32 v63, v64\n"
      "v_mov_b32 v64, v65\n"
      "v_subrev_u32 v65, s83, v52\n"
      "s_branch Ll%=\n"
      "Lx%=:\n"
      "s_lshl_b32 s83, s79, 3\n"
      "s_getpc_b64 s[94:95]\n"
      "Lp%=:\n"
      "s_add_u32 s94, s94, s83\n"
      "s_addc_u32 s95, s95, 0\n"
      "s_add_u32 s94, s94, Lt%=-Lp%=\n"
      "s_addc_u32 s95, s95, 0\n"
      "s_setpc_b64 s[94:95]\n"
      "Lt%=:\n"
      "s_waitcnt vmcnt(0)\n"
      "s_branch Ld%=\n"
      "s_waitcnt vmcnt(1)\n"
      "s_branch Ld%=\n"
      "s_waitcnt vmcnt(2)\n"
      "s_branch Ld%=\n"
      "s_waitcnt vmcnt(3)\n"
      "s_branch Ld%=\n"
      "s_waitcnt vmcnt(4)\n"
      "s_branch Ld%=\n"
      "s_waitcnt vmcnt(5)\n"
      "s_branch Ld%=\n"
      "s_waitcnt vmcnt(6)\n"
      "s_branch Ld%=\n"
      "s_waitcnt vmcnt(7)\n"
      "s_branch Ld%=\n"
      "s_waitcnt vmcnt(8)\n"
      "s_branch Ld%=\n"
      "s_waitcnt vmcnt(9)\n"
      "s_branch Ld%=\n"
      "s_waitcnt vmcnt(10)\n"
      "s_branch Ld%=\n"
      "s_waitcnt vmcnt(11)\n"
      "s_branch Ld%=\n"
      "s_waitcnt vmcnt(12)\n"
      "s_branch Ld%=\n"
      "s_waitcnt vmcnt(13)\n"
      "s_branch Ld%=\n"
      "s_waitcnt vmcnt(14)\n"
      "s_branch Ld%=\n"
      "s_waitcnt vmcnt(15)\n"
      "s_branch Ld%=\n"
      "s_waitcnt vmcnt(16)\n"
      "s_branch Ld%=\n"
      "s_waitcnt vmcnt(17)\n"
      "s_branch Ld%=\n"
      "s_waitcnt vmcnt(18)\n"
      "s_branch Ld%=\n"
      "s_waitcnt vmcnt(19)\n"
      "s_branch Ld%=\n"
      "s_waitcnt vmcnt(20)\n"
      "s_branch Ld%=\n"
      "s_waitcnt vmcnt(21)\n"
      "s_branch Ld%=\n"
      "s_waitcnt vmcnt(22)\n"
      "s_branch Ld%=\n"
      "s_waitcnt vmcnt(23)\n"
      "s_branch Ld%=\n"
      "Lf%=:\n"
      "s_cmp_lg_u32 s80, 0\n"
      "s_cbranch_scc1 Lb%=\n"
      "s_cmp_eq_u32 s81, 0\n"
      "s_cbranch_scc1 Lm%=\n"
      "v_cmp_ne_u64 vcc, v[48:49], v[56:57]\n"
      "v_cmp_ne_u64 s[92:93], v[50:51], v[56:57]\n"
      "s_or_b64 vcc, vcc, s[92:93]\n"
      "v_cmp_ne_u64 s[92:93], v[52:53], v[56:57]\n"
      "s_or_b64 vcc, vcc, s[92:93]\n"
      "v_cmp_ne_u64 s[92:93], v[54:55], v[56:57]\n"
      "s_or_b64 vcc, vcc, s[92:93]\n"
      "s_cmp_lg_u64 vcc, 0\n"
      "s_cselect_b32 s80, 0, 1\n"
      "s_cbranch_scc0 Lb%=\n"
      "Lm%=:\n"
      "s_cmp_eq_u32 s76, 21\n"
      "s_cbranch_scc1 Lb%=\n"
      "global_load_dwordx4 v[48:51], v40, %[ws] sc1\n"
      "global_load_dwordx4 v[52:55], v41, %[ws] sc1\n"
      "s_mov_b32 s81, 1\n"
      "s_branch Lb%=\n"
      "Lk%=:\n"
      "s_mov_b32 s78, 0\n"
      "s_cmp_eq_u32 s80, 1\n"
      "s_cbranch_scc0 Lq%=\n"
      "s_mov_b32 m0, %[qd]\n"
      "s_nop 0\n"
      "global_load_lds_dwordx4 v42, %[ws] offset:0 sc1\n"
      "global_load_lds_dwordx4 v43, %[ws] offset:1024 sc1\n"
      "global_load_lds_dwordx4 v44, %[ws] offset:2048 sc1\n"
      "s_add_u32 m0, %[kd], 76800\n"
      "s_nop 0\n"
      "global_load_lds_dwordx4 v60, %[kvs] offset:0 sc1\n"
      "global_load_lds_dwordx4 v61, %[kvs] offset:1024 sc1\n"
      "global_load_lds_dwordx4 v62, %[kvs] offset:2048 sc1\n"
      "global_load_lds_dwordx4 v63, %[kvs] offset:3072 sc1\n"
      "s_add_u32 m0, %[kd], 80896\n"
      "s_nop 0\n"
      "global_load_lds_dwordx4 v64, %[kvs] sc1\n"
      "s_mov_b64 exec, 0xffff\n"
      "global_load_lds_dwordx4 v65, %[kvs] offset:1024 sc1\n"
      "s_mov_b64 exec, -1\n"
      "s_mov_b32 s78, 9\n"
      "s_mov_b32 s80, 2\n"
      "s_add_u32 s79, s79, 9\n"
      "s_branch Ln%=\n"
      "Lq%=:\n"
      "s_cmp_eq_u32 s80, 2\n"
      "s_cbranch_scc0 Ln%=\n"
      "s_add_u32 m0, %[kd], 55296\n"
      "s_nop 0\n"
      "global_load_lds_dwordx4 v60, %[kv1] offset:0 sc1\n"
      "global_load_lds_dwordx4 v61, %[kv1] offset:1024 sc1\n"
      "global_load_lds_dwordx4 v62, %[kv1] offset:2048 sc1\n"
      "global_load_lds_dwordx4 v63, %[kv1] offset:3072 sc1\n"
      "s_add_u32 m0, %[kd], 59392\n"
      "s_nop 0\n"
      "global_load_lds_dwordx4 v64, %[kv1] sc1\n"
      "s_mov_b64 exec, 0xffff\n"
      "global_load_lds_dwordx4 v65, %[kv1] offset:1024 sc1\n"
      "s_mov_b64 exec, -1\n"
      "s_mov_b32 s78, 6\n"
      "s_mov_b32 s80, 3\n"
      "s_add_u32 s79, s79, 6\n"
      "s_branch Ln%=\n"
      "Le%=:\n"
      "s_max_u32 s80, s80, 1\n"
      "s_sub_u32 %[done], s80, 1\n"
      LOAD_OPS);
    } else {
      const int a = (idx - 30) * 8 + xcd;
      {
        const char* wsrc = (const char*)((a & 1) ? woB : woA) + (a >> 2) * 160 * 1280 + ((a >> 1) & 1) * 640;
        unsigned wo[5];
#pragma unroll
        for (int i = 0; i < 5; ++i) {
          const int c = i * 64 + lane, rw = c / 40;
          wo[i] = (unsigned)(rw * 1280 + (c - rw * 40) * 16);
          glds16(wsrc + (lw * 40 + 16) * 1280, wo[i] + (rw >> 2) * 16 * 1280, lds0 + (160 + lw * 8) * 640 + i * 1024);
        }
#pragma unroll
        for (int e = 0; e < 4; ++e) {
          const int row = (2 * e + (lw >> 1)) * 20 + (lw & 1) * 8;
#pragma unroll
          for (int i = 0; i < 5; ++i) glds16(wsrc + row * 1280, wo[i], lds0 + row * 640 + i * 1024);
        }
      }
      asm volatile("global_load_dwordx2 %0, %1, %2 sc1" : "=v"(tag) : "v"((unsigned)WS_EPOCH), "s"(ws) : "memory");
      unsigned ko[6];
#pragma unroll
      for (int i = 0; i < 6; ++i) {
        const unsigned p = lw * 336 + i * 64 + lane, tile = (p * 3121) >> 19, q = p - tile * 168, v = q - 80, b = (v * 745) >> 14, rw = v - b * 22, c = rw >> 2;
        ko[i] = (q < 80 ? tile * TS + head * TQ + q * 16
               : rw < 20 ? tile * TS + TS_V + b * 10240 + (head * 20 + ((c == 1 ? 2 : c == 2 ? 1 : c) << 2) + (rw & 3)) * 16 : TS_C + (head >> 1) * 32 + (rw - 20) * 16) - (i & 3) * 1024;
      }
      ka = u32x4{ko[0], ko[1], ko[2], ko[3]};
      kb = u32x2{ko[4], ko[5]};
      WAIT_BAR(21);
      WAIT_BAR(16);
      WAIT_BAR(11);
      WAIT_BAR(6);
      WAIT_BAR(0);
      asm volatile("" : "+v"(tag));
      tag += TAG_STEP;
      if (tid < 514) set_flag(ws + WS_FX + a * 16 + lane * 8, tag);
      if (tid == 512) *(LAS u64*)((lds_ptr)lds + SY_TAG) = tag;
      LBAR();
    }
    done = uni(done);
    if (done < 1) {
      poll_pairs(ws, fb1, tag); poll_pairs(ws, fb2, tag);
      asm volatile("s_mov_b32 m0, %4\n\ts_nop 0\n\tglobal_load_lds_dwordx4 %0, %3 sc1\n\tglobal_load_lds_dwordx4 %1, %3 offset:1024 sc1\n\t"
                   "global_load_lds_dwordx4 %2, %3 offset:2048 sc1" ::"v"(qo[0]), "v"(qo[1]), "v"(qo[2]), "s"(ws), "s"(lds0 + L_Q + lw * 3072) : "memory");
    }
#pragma nounroll
    for (int st = done; st < 2; ++st) kv_dma(st);
    VMWAIT(6);
    lds_add(lds0 + SY_LAND, lane == 0);
    const char* wimg = ws + WS_W + (size_t)cb3 * 204800;
    auto w_dma = [&](int hf, int at) {
      const char* src = wimg + hf * 51200 + lw * 12800;
      const unsigned dst = lds0 + at + lw * 12800;
#pragma unroll
      for (int i = 0; i < 3; ++i) glds64c(src + i * 4096, lane * 16, dst + i * 4096);
      if (lane < 32) glds16c(src + 12288, lane * 16, dst + 12288);
    };
    const unsigned sy = lds0 + L_SY, pga = lds0 + SY_PROG + (lane & 7) * 4;
    unsigned lb = 0;
    auto lsync = [&]() { lds_add(lds0 + SY_LBAR, lane == 0); lb += 4; lds_wait(lds0 + SY_LBAR, lb); };
    __builtin_amdgcn_s_setprio(2);
    const int t0 = qb * 8 + lw * 2;
    const bf16x8 f1 = cat8(ld8c(ws + WS_PHI + head * 2048 + lane * 16), ld8c(ws + WS_PHI + head * 2048 + lane * 16 + 8));
    const s16x4 f2 = ld8c(ws + WS_PHI + head * 2048 + 1024 + lane * 16);
    VMWAIT(0);
    lds_add(sy + 4, lane == 0);
    lds_wait<2>(pga + 64, 2u);
    char* sl = ws + WS_SLAB + (size_t)(head * 8 + qb) * 6144 + lane * 16;
    st16(sl + lw * 1024, u32x4{~0u, ~0u, ~0u, ~0u});
    if (lw < 2) st16(sl + (lw + 4) * 1024, u32x4{~0u, ~0u, ~0u, ~0u});
    kv_dma(2);
    VMWAIT(0);
    if (lane == 0) set_flag(ws + WS_FKV + head * 256 + (qb * 4 + lw) * 8, tag);
    lds_add(sy + 8, lane == 0);
    kv_dma(3);
    poll_flags(lane < 48 ? ws + WS_FA + ((640 * g4 + 20 * head + (lane & 1) * 19) >> 5) * 256 + (qb * 4 + ((lane >> 1) & 3)) * 8 : ws + WS_FW + ((l16 >> 3) * 64 + qb * 8 + (lane & 7)) * 8, tag);
    lds_add(sy + 12, lane == 0);
    constexpr int L_KA = kv_slot(5), L_QA = kv_slot(4);
    kv_copy(ws + WS_KVA + qb * 8 * TS, L_KA);
    {
      const char* qa = ws + WS_QA + (size_t)(head * 64 + t0) * TQ;
      glds16c(qa, lane * 16, lds0 + L_QA + lw * 2560);
      glds16c(qa + 1024, lane * 16, lds0 + L_QA + lw * 2560 + 1024);
      if (lane < 32) glds16c(qa + 2048, lane * 16, lds0 + L_QA + lw * 2560 + 2048);
    }
    float gate;
    asm volatile("global_load_dword %0, %1, %2 sc1" : "=v"(gate) : "v"((unsigned)WS_G + (t0 * 32 + lane) * 4), "s"(ws) : "memory");
    asm volatile("s_waitcnt vmcnt(0)" : "+v"(gate)::"memory");
    bf16x8 xq1[2];
    s16x4 xq2[2];
#pragma unroll
    for (int i = 0; i < 2; ++i) {
      const lds_cptr qa = (lds_cptr)lds + L_QA + (lw * 2 + i) * TQ + posq;
      xq1[i] = cat8(*(LAS const s16x4*)(qa + h * 512), *(LAS const s16x4*)(qa + h * 512 + 256));
      xq2[i] = *(LAS const s16x4*)(qa + 1024);
    }
    {
      u32x4 phk[2][3];
      bf16x8 xk1[2];
      s16x4 xk2[2];
#pragma unroll
      for (int i = 0; i < 2; ++i) {
        const lds_cptr ka = (lds_cptr)lds + L_KA + (lw * 2 + i) * TKV + posk;
        xk1[i] = cat8(*(LAS const s16x4*)(ka + h * 512), *(LAS const s16x4*)(ka + h * 512 + 256));
        xk2[i] = *(LAS const s16x4*)(ka + 1024);
      }
      featmap<true>(f1, f2, xk1, xk2, phk);
      const lds_ptr pw = (lds_ptr)lds + L_PHIK + (lw * 64 + r) * 96 + h * 16;
#pragma unroll
      for (int j = 0; j < 6; ++j) *(LAS u32x4*)(pw + (j / 3) * 3072 + (j % 3) * 32) = phk[j / 3][j % 3];
    }
    if (lane < 24) *(LAS unsigned*)((lds_ptr)lds + L_KVB + 3072 + lane * 4) = 0x3F803F80u;
    lsync();
    kv_dma(4);
    {
      const lds_cptr pr = (lds_cptr)lds + L_PHIK + (16 * (g4 >> 1) + 4 * (g4 & 1) + (l16 >> 2)) * 96 + (l16 & 3) * 8;
      const lds_cptr va = (lds_cptr)lds + L_KA + TQ + g4 * VB + ((lw & 1) * 16 + l16 < 21 ? (lw & 1) * 16 + l16 : 21) * 16;
      bf16x8 vf[8], pa[8], pb[8];
#pragma unroll
      for (int t = 0; t < 8; ++t) {
        const lds_cptr p = pr + t * 32 * 96;
        vf[t] = *(LAS const bf16x8*)(va + t * TKV);
        pa[t] = cat8(ldtr(p + (lw >> 1) * 32), ldtr(p + (lw >> 1) * 32 + 8 * 96));
        pb[t] = cat8(ldtr(p + 64), ldtr(p + 64 + 8 * 96));
      }
      asm volatile("" : "+v"(vf[0]), "+v"(vf[1]), "+v"(vf[2]), "+v"(vf[3]), "+v"(pa[0]), "+v"(pa[1]), "+v"(pa[2]), "+v"(pa[3]), "+v"(pb[0]), "+v"(pb[1]), "+v"(pb[2]), "+v"(pb[3]),
                   "+v"(vf[4]), "+v"(vf[5]), "+v"(vf[6]), "+v"(vf[7]), "+v"(pa[4]), "+v"(pa[5]), "+v"(pa[6]), "+v"(pa[7]), "+v"(pb[4]), "+v"(pb[5]), "+v"(pb[6]), "+v"(pb[7]));
      f32x4 ka = {}, kb = {};
#pragma unroll
      for (int t = 0; t < 8; ++t) { ka = MFMA16(vf[t], pa[t], ka); kb = MFMA16(vf[t], pb[t], kb); }
      const u32x4 lim = {0xFFFFFFFEu, 0xFFFFFFFEu, 0xFFFFFFFEu, 0xFFFFFFFEu};
      st16(sl + lw * 1024, __builtin_elementwise_min(__builtin_bit_cast(u32x4, ka), lim));
      if (lw < 2) st16(sl + (lw + 4) * 1024, __builtin_elementwise_min(__builtin_bit_cast(u32x4, kb), lim));
    }
    lsync();
    const char* fk = ws + WS_FKV + head * 256 + r * 8;
    u64 fkv;
    asm volatile("global_load_dwordx2 %0, %1, off sc1" : "=v"(fkv) : "v"(fk) : "memory");
    kv_dma(5);
    u32x4 phq[2][3];
    featmap<false>(f1, f2, xq1, xq2, phq);
    asm volatile("" : "+v"(phq[0][0]), "+v"(phq[0][1]), "+v"(phq[0][2]), "+v"(phq[1][0]), "+v"(phq[1][1]), "+v"(phq[1][2])::"memory");
    asm volatile("s_waitcnt vmcnt(6)" : "+v"(fkv)::"memory");
    lds_add(sy + 16, lane == 0);
    if (!__all(fkv == tag)) poll_flags(fk, tag);
    lds_wait<4>(pga, sy + 8);
    {
      f32x4 part[12];
      const char* sa = ws + WS_SLAB + (size_t)head * 8 * 6144;
      const unsigned s0 = lw * 1024 + lane * 16, s1 = h * 6144 + (4 + (lw & 1)) * 1024 + ((lw >> 1) * 32 + r) * 16;
#pragma nounroll
      for (unsigned spins = 0; spins < SPIN_CAP; ++spins) {
#pragma unroll
        for (int i = 0; i < 8; ++i) asm volatile("global_load_dwordx4 %0, %1, %2 sc1" : "=v"(part[i]) : "v"(s0), "s"(sa + i * 6144) : "memory");
#pragma unroll
        for (int i = 0; i < 4; ++i) asm volatile("global_load_dwordx4 %0, %1, %2 sc1" : "=v"(part[8 + i]) : "v"(s1), "s"(sa + i * 12288) : "memory");
        if (spins == 0) {
#pragma nounroll
          for (int st = 6; st < 8; ++st) kv_dma(st);
          VMWAIT(24);
          lds_add(sy + 20, lane == 0);
          VMWAIT(12);
        } else {
          VMWAIT(0);
        }
        asm volatile("" : "+v"(part[0]), "+v"(part[1]), "+v"(part[2]), "+v"(part[3]), "+v"(part[4]), "+v"(part[5]), "+v"(part[6]), "+v"(part[7]),
                     "+v"(part[8]), "+v"(part[9]), "+v"(part[10]), "+v"(part[11])::"memory");
        unsigned m = 0;
#pragma unroll
        for (int i = 0; i < 12; ++i) {
          const u32x4 w = __builtin_bit_cast(u32x4, part[i]);
          m = umax3(m, umax3(w[0], w[1], w[2]), w[3]);
        }
        if (__all(m != ~0u)) break;
      }
      const f32x4 t = ((part[0] + part[1]) + (part[2] + part[3])) + ((part[4] + part[5]) + (part[6] + part[7]));
      const f32x4 u = (part[8] + part[9]) + (part[10] + part[11]);
      const lds_ptr kw = (lds_ptr)lds + L_KVB + ((lw & 1) * 16 + g4 * 4) * 96 + ((lw >> 1) * 16 + l16) * 2;
      unsigned a = pk2(t[0], t[1]), b = pk2(t[2], t[3]);
      *(LAS u16*)(kw) = (u16)a; *(LAS u16*)(kw + 96) = (u16)(a >> 16); *(LAS u16*)(kw + 192) = (u16)b; *(LAS u16*)(kw + 288) = (u16)(b >> 16);
      const lds_ptr kx = (lds_ptr)lds + L_KVB + ((lw & 1) * 16 + ((lw >> 1) * 2 + (g4 & 1)) * 4 + h * 2) * 96 + (32 + l16) * 2;
      const f32x2 u0 = swap32(u[0], u[2]), u1 = swap32(u[1], u[3]);
      a = pk2(u0[0] + u0[1], u1[0] + u1[1]);
      *(LAS u16*)(kx) = (u16)a; *(LAS u16*)(kx + 96) = (u16)(a >> 16);
    }
    lsync();
    {
      const lds_cptr kr = (lds_cptr)lds + L_KVB + ((r & 3) == 1 && r >= 24 ? 32 : vslot) * 96 + h * 16;
      const bf16x8 k0 = *(LAS const bf16x8*)(kr), k2 = *(LAS const bf16x8*)(kr + 64);
      const u32x4 k1 = *(LAS const u32x4*)(kr + 32);
      const f32x2 gt = swap32(gate, gate);
#pragma unroll
      for (int i = 0; i < 2; ++i) {
        unsigned oa[6];
        f32x16 np = MFMA32(k0, BF8(phq[i][0]), f32x16{}), nm = MFMA32(k2, BF8(phq[i][2]), f32x16{});
        np = MFMA8(BF4((u32x2{k1[0], k1[1]})), BF4((u32x2{phq[i][1][0], phq[i][1][1]})), np);
        nm = MFMA8(BF4((u32x2{k1[2], k1[3]})), BF4((u32x2{phq[i][1][2], phq[i][1][3]})), nm);
        const float rp = __builtin_amdgcn_rcpf(np[13]), rm = __builtin_amdgcn_rcpf(nm[13]);
        const float sc2 = gt[i] * __builtin_amdgcn_rcpf(np[12] * rp + nm[12] * rm + 1e-6f), cp = rp * sc2, cm = rm * sc2;
#pragma unroll
        for (int j = 0; j < 6; ++j) oa[j] = pk2(np[2 * j] * cp + nm[2 * j] * cm, np[2 * j + 1] * cp + nm[2 * j + 1] * cm);
        store_o(ws + WS_OA, t0 + i, head, u32x4{oa[0], oa[1], oa[2], oa[3]}, u32x2{oa[4], oa[5]}, r, h);
        store_o(ws + WS_OB, t0 + i, head, u32x4{~0u, ~0u, ~0u, ~0u}, u32x2{~0u, ~0u}, r, h);
      }
    }
    const char* fw = ws + WS_FW + ((((lane >> 3) & 1) + (cb3 >> 2) * 2 + g4 * 4) * 8 + (lane & 7)) * 8;
    u64 fwv;
    asm volatile("global_load_dwordx2 %0, %1, off sc1\n\ts_waitcnt vmcnt(0)" : "=v"(fwv) : "v"(fw) : "memory");
    if (lane == 0) set_flag(ws + WS_FOA + (qb * 4 + lw) * 256 + head * 8, tag);
    lds_add(sy + 24 + lane * 4, lane < 2);
    if (!__all(fwv == tag)) poll_flags(fw, tag);
    lds_wait<4>(pga, sy + 12);
#define OUTT 2
    if (lw < OUTT) {
      touch(out + (size_t)rb3 * 64 * D + cb3 * 80, lane * (D * 2) + lw * 156, lds0 + L_SY + 512 + lw * 256);
    }
    lsync();
    w_dma(0, L_W0);
    poll_flags(ws + WS_FOA + rb3 * 256 + r * 8, tag);
    if (blockIdx.x == 0 && tid == 512) set_flag(ws + WS_EPOCH, tag);
    {
      const u32x8 in = {(unsigned)lane * 16, (unsigned)(4096 + lane * 16), (unsigned)(16384 + g4 * 256 + l16 * 8), lds0 + L_W0 + lane * 16, pga, lds0 + L_G + lw * 256 + lane * 4,
                        __float_as_uint(gate), lds0 + L_APART + lw * 5120 + lane * 16};
      const char* src = ws + WS_OA + (size_t)(rb3 * 4 + lw) * 20480;
      const u64 wsrc = (u64)(wimg + lw * 12800);
      int sp;
      asm volatile(
      "v_mov_b32 v141, 1\n"
      "v_mov_b32 v142, %[bar]\n"
      "s_waitcnt lgkmcnt(0)\n"
      "s_mov_b64 exec, 1\n"
      "ds_add_u32 v142, v141 offset:0\n"
      "s_mov_b64 exec, -1\n"
      "s_add_u32 %[lb], %[lb], 4\n"
      "global_load_dwordx4 v[20:23], v145, %[src] offset:-4096 sc1\n"
      "global_load_dwordx4 v[24:27], v145, %[src] offset:-3072 sc1\n"
      "global_load_dwordx4 v[28:31], v145, %[src] offset:-2048 sc1\n"
      "global_load_dwordx4 v[32:35], v145, %[src] offset:-1024 sc1\n"
      "global_load_dwordx4 v[36:39], v145, %[src] offset:0 sc1\n"
      "global_load_dwordx4 v[40:43], v145, %[src] offset:1024 sc1\n"
      "global_load_dwordx4 v[44:47], v145, %[src] offset:2048 sc1\n"
      "global_load_dwordx4 v[48:51], v145, %[src] offset:3072 sc1\n"
      "global_load_dwordx2 v[52:53], v146, %[src] offset:0 sc1\n"
      "global_load_dwordx2 v[54:55], v146, %[src] offset:128 sc1\n"
      "global_load_dwordx2 v[56:57], v146, %[src] offset:1024 sc1\n"
      "global_load_dwordx2 v[58:59], v146, %[src] offset:1152 sc1\n"
      "global_load_dwordx4 v[60:63], v145, %[sr2] offset:-4096 sc1\n"
      "global_load_dwordx4 v[64:67], v145, %[sr2] offset:-3072 sc1\n"
      "global_load_dwordx4 v[68:71], v145, %[sr2] offset:-2048 sc1\n"
      "global_load_dwordx4 v[72:75], v145, %[sr2] offset:-1024 sc1\n"
      "global_load_dwordx4 v[76:79], v145, %[sr2] offset:0 sc1\n"
      "global_load_dwordx4 v[80:83], v145, %[sr2] offset:1024 sc1\n"
      "global_load_dwordx4 v[84:87], v145, %[sr2] offset:2048 sc1\n"
      "global_load_dwordx4 v[88:91], v145, %[sr2] offset:3072 sc1\n"
      "global_load_dwordx2 v[92:93], v146, %[src] offset:2048 sc1\n"
      "global_load_dwordx2 v[94:95], v146, %[src] offset:2176 sc1\n"
      "global_load_dwordx2 v[96:97], v146, %[src] offset:3072 sc1\n"
      "global_load_dwordx2 v[98:99], v146, %[src] offset:3200 sc1\n"
      "s_add_u32 s92, %[bar], -72\n"
      "s_mov_b32 %[sp], %[cap]\n"
      "Lw1%=:\n"
      "ds_read_b32 v140, v142\n"
      "s_sub_u32 %[sp], %[sp], 1\n"
      "s_waitcnt lgkmcnt(0)\n"
      "v_cmp_gt_u32 vcc, %[lb], v140\n"
      "s_cbranch_vccz Lx1%=\n"
      "s_sleep 1\n"
      "s_cmp_lg_u32 %[sp], 0\n"
      "s_cbranch_scc1 Lw1%=\n"
      "Lx1%=:\n"
      "ds_read_b128 v[100:103], v147 offset:0\n"
      "ds_read_b128 v[104:107], v147 offset:1024\n"
      "ds_read_b128 v[108:111], v147 offset:2048\n"
      "ds_read_b128 v[112:115], v147 offset:3072\n"
      "ds_read_b128 v[116:119], v147 offset:4096\n"
      "ds_read_b128 v[120:123], v147 offset:5120\n"
      "ds_read_b128 v[124:127], v147 offset:6144\n"
      "ds_read_b128 v[128:131], v147 offset:7168\n"
      "ds_read_b128 v[132:135], v147 offset:8192\n"
      "s_setprio 0\n"
      "ds_read_b128 v[136:139], v147 offset:9216\n"
      "s_waitcnt vmcnt(23) lgkmcnt(9)\n"
      "v_mfma_f32_16x16x32_bf16 v[0:3], v[100:103], v[20:23], 0\n"
      "ds_read_b128 v[100:103], v147 offset:10240\n"
      "s_waitcnt lgkmcnt(9)\n"
      "v_mfma_f32_16x16x32_bf16 v[4:7], v[104:107], v[20:23], 0\n"
      "ds_read_b128 v[104:107], v147 offset:11264\n"
      "s_waitcnt lgkmcnt(9)\n"
      "v_mfma_f32_16x16x32_bf16 v[8:11], v[108:111], v[20:23], 0\n"
      "ds_read_b128 v[108:111], v147 offset:12288\n"
      "s_waitcnt lgkmcnt(9)\n"
      "v_mfma_f32_16x16x32_bf16 v[12:15], v[112:115], v[20:23], 0\n"
      "ds_read_b128 v[112:115], v147 offset:13312\n"
      "s_waitcnt lgkmcnt(9)\n"
      "v_mfma_f32_16x16x32_bf16 v[16:19], v[116:119], v[20:23], 0\n"
      "ds_read_b128 v[116:119], v147 offset:14336\n"
      "s_waitcnt vmcnt(22) lgkmcnt(9)\n"
      "v_mfma_f32_16x16x32_bf16 v[0:3], v[120:123], v[24:27], v[0:3]\n"
      "ds_read_b128 v[120:123], v147 offset:15360\n"
      "s_waitcnt lgkmcnt(9)\n"
      "v_mfma_f32_16x16x32_bf16 v[4:7], v[124:127], v[24:27], v[4:7]\n"
      "ds_read_b128 v[124:127], v147 offset:16384\n"
      "s_waitcnt lgkmcnt(9)\n"
      "v_mfma_f32_16x16x32_bf16 v[8:11], v[128:131], v[24:27], v[8:11]\n"
      "ds_read_b128 v[128:131], v147 offset:17408\n"
      "s_waitcnt lgkmcnt(9)\n"
      "v_mfma_f32_16x16x32_bf16 v[12:15], v[132:135], v[24:27], v[12:15]\n"
      "ds_read_b128 v[132:135], v147 offset:18432\n"
      "s_waitcnt lgkmcnt(9)\n"
      "v_mfma_f32_16x16x32_bf16 v[16:19], v[136:139], v[24:27], v[16:19]\n"
      "ds_read_b128 v[136:139], v147 offset:19456\n"
      "s_waitcnt vmcnt(21) lgkmcnt(9)\n"
      "v_mfma_f32_16x16x32_bf16 v[0:3], v[100:103], v[28:31], v[0:3]\n"
      "ds_read_b128 v[100:103], v147 offset:20480\n"
      "s_waitcnt lgkmcnt(9)\n"
      "v_mfma_f32_16x16x32_bf16 v[4:7], v[104:107], v[28:31], v[4:7]\n"
      "ds_read_b128 v[104:107], v147 offset:21504\n"
      "s_waitcnt lgkmcnt(9)\n"
      "v_mfma_f32_16x16x32_bf16 v[8:11], v[108:111], v[28:31], v[8:11]\n"
      "ds_read_b128 v[108:111], v147 offset:22528\n"
      "s_waitcnt lgkmcnt(9)\n"
      "v_mfma_f32_16x16x32_bf16 v[12:15], v[112:115], v[28:31], v[12:15]\n"
      "ds_read_b128 v[112:115], v147 offset:23552\n"
      "s_waitcnt lgkmcnt(9)\n"
      "v_mfma_f32_16x16x32_bf16 v[16:19], v[116:119], v[28:31], v[16:19]\n"
      "ds_read_b128 v[116:119], v147 offset:24576\n"
      "s_waitcnt vmcnt(20) lgkmcnt(9)\n"
      "v_mfma_f32_16x16x32_bf16 v[0:3], v[120:123], v[32:35], v[0:3]\n"
      "ds_read_b128 v[120:123], v147 offset:25600\n"
      "s_waitcnt lgkmcnt(9)\n"
      "v_mfma_f32_16x16x32_bf16 v[4:7], v[124:127], v[32:35], v[4:7]\n"
      "ds_read_b128 v[124:127], v147 offset:26624\n"
      "s_waitcnt lgkmcnt(9)\n"
      "v_mfma_f32_16x16x32_bf16 v[8:11], v[128:131], v[32:35], v[8:11]\n"
      "ds_read_b128 v[128:131], v147 offset:27648\n"
      "s_waitcnt lgkmcnt(9)\n"
      "v_mfma_f32_16x16x32_bf16 v[12:15], v[132:135], v[32:35], v[12:15]\n"
      "ds_read_b128 v[132:135], v147 offset:28672\n"
      "s_waitcnt lgkmcnt(9)\n"
      "v_mfma_f32_16x16x32_bf16 v[16:19], v[136:139], v[32:35], v[16:19]\n"
      "ds_read_b128 v[136:139], v147 offset:29696\n"
      "s_waitcnt vmcnt(19) lgkmcnt(9)\n"
      "v_mfma_f32_16x16x32_bf16 v[0:3], v[100:103], v[36:39], v[0:3]\n"
      "ds_read_b128 v[100:103], v147 offset:30720\n"
      "s_waitcnt lgkmcnt(9)\n"
      "v_mfma_f32_16x16x32_bf16 v[4:7], v[104:107], v[36:39], v[4:7]\n"
      "ds_read_b128 v[104:107], v147 offset:31744\n"
      "s_waitcnt lgkmcnt(9)\n"
      "v_mfma_f32_16x16x32_bf16 v[8:11], v[108:111], v[36:39], v[8:11]\n"
      "ds_read_b128 v[108:111], v147 offset:32768\n"
      "s_waitcnt lgkmcnt(9)\n"
      "v_mfma_f32_16x16x32_bf16 v[12:15], v[112:115], v[36:39], v[12:15]\n"
      "ds_read_b128 v[112:115], v147 offset:33792\n"
      "s_waitcnt lgkmcnt(9)\n"
      "v_mfma_f32_16x16x32_bf16 v[16:19], v[116:119], v[36:39], v[16:19]\n"
      "ds_read_b128 v[116:119], v147 offset:34816\n"
      "s_waitcnt vmcnt(18) lgkmcnt(9)\n"
      "v_mfma_f32_16x16x32_bf16 v[0:3], v[120:123], v[40:43], v[0:3]\n"
      "ds_read_b128 v[120:123], v147 offset:35840\n"
      "s_waitcnt lgkmcnt(9)\n"
      "v_mfma_f32_16x16x32_bf16 v[4:7], v[124:127], v[40:43], v[4:7]\n"
      "ds_read_b128 v[124:127], v147 offset:36864\n"
      "s_waitcnt lgkmcnt(9)\n"
      "v_mfma_f32_16x16x32_bf16 v[8:11], v[128:131], v[40:43], v[8:11]\n"
      "ds_read_b128 v[128:131], v147 offset:37888\n"
      "s_waitcnt lgkmcnt(9)\n"
      "v_mfma_f32_16x16x32_bf16 v[12:15], v[132:135], v[40:43], v[12:15]\n"
      "ds_read_b128 v[132:135], v147 offset:38912\n"
      "s_waitcnt lgkmcnt(9)\n"
      "v_mfma_f32_16x16x32_bf16 v[16:19], v[136:139], v[40:43], v[16:19]\n"
      "ds_read_b128 v[136:139], v147 offset:39936\n"
      "s_waitcnt vmcnt(17) lgkmcnt(9)\n"
      "v_mfma_f32_16x16x32_bf16 v[0:3], v[100:103], v[44:47], v[0:3]\n"
      "ds_read_b128 v[100:103], v147 offset:40960\n"
      "s_waitcnt lgkmcnt(9)\n"
      "v_mfma_f32_16x16x32_bf16 v[4:7], v[104:107], v[44:47], v[4:7]\n"
      "ds_read_b128 v[104:107], v147 offset:41984\n"
      "s_waitcnt lgkmcnt(9)\n"
      "v_mfma_f32_16x16x32_bf16 v[8:11], v[108:111], v[44:47], v[8:11]\n"
      "ds_read_b128 v[108:111], v147 offset:43008\n"
      "s_waitcnt lgkmcnt(9)\n"
      "v_mfma_f32_16x16x32_bf16 v[12:15], v[112:115], v[44:47], v[12:15]\n"
      "ds_read_b128 v[112:115], v147 offset:44032\n"
      "s_waitcnt lgkmcnt(9)\n"
      "v_mfma_f32_16x16x32_bf16 v[16:19], v[116:119], v[44:47], v[16:19]\n"
      "ds_read_b128 v[116:119], v147 offset:45056\n"
      "s_waitcnt vmcnt(16) lgkmcnt(9)\n"
      "v_mfma_f32_16x16x32_bf16 v[0:3], v[120:123], v[48:51], v[0:3]\n"
      "ds_read_b128 v[120:123], v147 offset:46080\n"
      "s_waitcnt lgkmcnt(9)\n"
      "v_mfma_f32_16x16x32_bf16 v[4:7], v[124:127], v[48:51], v[4:7]\n"
      "ds_read_b128 v[124:127], v147 offset:47104\n"
      "s_waitcnt lgkmcnt(9)\n"
      "v_mfma_f32_16x16x32_bf16 v[8:11], v[128:131], v[48:51], v[8:11]\n"
      "ds_read_b128 v[128:131], v147 offset:48128\n"
      "s_waitcnt lgkmcnt(9)\n"
      "v_mfma_f32_16x16x32_bf16 v[12:15], v[132:135], v[48:51], v[12:15]\n"
      "ds_read_b128 v[132:135], v147 offset:49152\n"
      "s_waitcnt lgkmcnt(9)\n"
      "v_mfma_f32_16x16x32_bf16 v[16:19], v[136:139], v[48:51], v[16:19]\n"
      "ds_read_b128 v[136:139], v147 offset:50176\n"
      "s_waitcnt vmcnt(14) lgkmcnt(9)\n"
      "v_mfma_f32_16x16x32_bf16 v[0:3], v[100:103], v[52:55], v[0:3]\n"
      "s_waitcnt lgkmcnt(8)\n"
      "v_mfma_f32_16x16x32_bf16 v[4:7], v[104:107], v[52:55], v[4:7]\n"
      "s_waitcnt lgkmcnt(7)\n"
      "v_mfma_f32_16x16x32_bf16 v[8:11], v[108:111], v[52:55], v[8:11]\n"
      "s_waitcnt lgkmcnt(6)\n"
      "v_mfma_f32_16x16x32_bf16 v[12:15], v[112:115], v[52:55], v[12:15]\n"
      "s_waitcnt lgkmcnt(5)\n"
      "v_mfma_f32_16x16x32_bf16 v[16:19], v[116:119], v[52:55], v[16:19]\n"
      "s_waitcnt vmcnt(12) lgkmcnt(4)\n"
      "v_mfma_f32_16x16x32_bf16 v[0:3], v[120:123], v[56:59], v[0:3]\n"
      "s_waitcnt lgkmcnt(3)\n"
      "v_mfma_f32_16x16x32_bf16 v[4:7], v[124:127], v[56:59], v[4:7]\n"
      "s_waitcnt lgkmcnt(2)\n"
      "v_mfma_f32_16x16x32_bf16 v[8:11], v[128:131], v[56:59], v[8:11]\n"
      "s_waitcnt lgkmcnt(1)\n"
      "v_mfma_f32_16x16x32_bf16 v[12:15], v[132:135], v[56:59], v[12:15]\n"
      "s_waitcnt lgkmcnt(0)\n"
      "v_mfma_f32_16x16x32_bf16 v[16:19], v[136:139], v[56:59], v[16:19]\n"
      "s_setprio 2\n"
      "s_waitcnt lgkmcnt(0)\n"
      "s_mov_b64 exec, 1\n"
      "ds_add_u32 v142, v141 offset:0\n"
      "s_mov_b64 exec, -1\n"
      "s_add_u32 %[lb], %[lb], 4\n"
      "s_mov_b32 %[sp], %[cap]\n"
      "Lw5%=:\n"
      "ds_read_b32 v140, v142\n"
      "s_sub_u32 %[sp], %[sp], 1\n"
      "s_waitcnt lgkmcnt(0)\n"
      "v_cmp_gt_u32 vcc, %[lb], v140\n"
      "s_cbranch_vccz Lx5%=\n"
      "s_sleep 1\n"
      "s_cmp_lg_u32 %[sp], 0\n"
      "s_cbranch_scc1 Lw5%=\n"
      "Lx5%=:\n"
      "s_add_u32 s90, %[wl], 51200\n"
      "s_addc_u32 s91, %[wh], 0\n"
      "s_add_u32 m0, %[d0], 0\n"
      "s_nop 0\n"
      "global_load_lds_dwordx4 v144, s[90:91] offset:0 sc1\n"
      "global_load_lds_dwordx4 v144, s[90:91] offset:1024 sc1\n"
      "global_load_lds_dwordx4 v144, s[90:91] offset:2048 sc1\n"
      "global_load_lds_dwordx4 v144, s[90:91] offset:3072 sc1\n"
      "s_add_u32 s90, s90, 4096\n"
      "s_addc_u32 s91, s91, 0\n"
      "s_add_u32 m0, %[d0], 4096\n"
      "s_nop 0\n"
      "global_load_lds_dwordx4 v144, s[90:91] offset:0 sc1\n"
      "global_load_lds_dwordx4 v144, s[90:91] offset:1024 sc1\n"
      "global_load_lds_dwordx4 v144, s[90:91] offset:2048 sc1\n"
      "global_load_lds_dwordx4 v144, s[90:91] offset:3072 sc1\n"
      "s_add_u32 s90, s90, 4096\n"
      "s_addc_u32 s91, s91, 0\n"
      "s_add_u32 m0, %[d0], 8192\n"
      "s_nop 0\n"
      "global_load_lds_dwordx4 v144, s[90:91] offset:0 sc1\n"
      "global_load_lds_dwordx4 v144, s[90:91] offset:1024 sc1\n"
      "global_load_lds_dwordx4 v144, s[90:91] offset:2048 sc1\n"
      "global_load_lds_dwordx4 v144, s[90:91] offset:3072 sc1\n"
      "s_add_u32 s90, s90, 4096\n"
      "s_addc_u32 s91, s91, 0\n"
      "s_add_u32 m0, %[d0], 12288\n"
      "s_mov_b32 exec_hi, 0\n"
      "global_load_lds_dwordx4 v144, s[90:91] sc1\n"
      "s_mov_b32 exec_hi, -1\n"
      "ds_read_b32 v140, v148\n"
      "s_mov_b32 s93, 0\n"
      "s_waitcnt lgkmcnt(0)\n"
      "v_cmp_gt_u32 vcc, s92, v140\n"
      "s_cbranch_vccnz Ln%=\n"
      "s_add_u32 s90, %[wl], 153600\n"
      "s_addc_u32 s91, %[wh], 0\n"
      "s_add_u32 m0, %[d1], 0\n"
      "s_nop 0\n"
      "global_load_lds_dwordx4 v144, s[90:91] offset:0 sc1\n"
      "global_load_lds_dwordx4 v144, s[90:91] offset:1024 sc1\n"
      "global_load_lds_dwordx4 v144, s[90:91] offset:2048 sc1\n"
      "global_load_lds_dwordx4 v144, s[90:91] offset:3072 sc1\n"
      "s_add_u32 s90, s90, 4096\n"
      "s_addc_u32 s91, s91, 0\n"
      "s_add_u32 m0, %[d1], 4096\n"
      "s_nop 0\n"
      "global_load_lds_dwordx4 v144, s[90:91] offset:0 sc1\n"
      "global_load_lds_dwordx4 v144, s[90:91] offset:1024 sc1\n"
      "global_load_lds_dwordx4 v144, s[90:91] offset:2048 sc1\n"
      "global_load_lds_dwordx4 v144, s[90:91] offset:3072 sc1\n"
      "s_add_u32 s90, s90, 4096\n"
      "s_addc_u32 s91, s91, 0\n"
      "s_add_u32 m0, %[d1], 8192\n"
      "s_nop 0\n"
      "global_load_lds_dwordx4 v144, s[90:91] offset:0 sc1\n"
      "global_load_lds_dwordx4 v144, s[90:91] offset:1024 sc1\n"
      "global_load_lds_dwordx4 v144, s[90:91] offset:2048 sc1\n"
      "global_load_lds_dwordx4 v144, s[90:91] offset:3072 sc1\n"
      "s_add_u32 s90, s90, 4096\n"
      "s_addc_u32 s91, s91, 0\n"
      "s_add_u32 m0, %[d1], 12288\n"
      "s_mov_b32 exec_hi, 0\n"
      "global_load_lds_dwordx4 v144, s[90:91] sc1\n"
      "s_mov_b32 exec_hi, -1\n"
      "ds_write_b32 v149, v150\n"
      "s_waitcnt lgkmcnt(0)\n"
      "s_mov_b64 exec, 1\n"
      "ds_add_u32 v142, v141 offset:8\n"
      "s_mov_b64 exec, -1\n"
      "s_mov_b32 s93, 1\n"
      "s_waitcnt vmcnt(13)\n"
      "s_branch Lo%=\n"
      "Ln%=:\n"
      "s_waitcnt vmcnt(0)\n"
      "Lo%=:\n"
      "s_waitcnt lgkmcnt(0)\n"
      "s_mov_b64 exec, 1\n"
      "ds_add_u32 v142, v141 offset:0\n"
      "s_mov_b64 exec, -1\n"
      "s_add_u32 %[lb], %[lb], 4\n"
      "s_mov_b32 %[sp], %[cap]\n"
      "Lw6%=:\n"
      "ds_read_b32 v140, v142\n"
      "s_sub_u32 %[sp], %[sp], 1\n"
      "s_waitcnt lgkmcnt(0)\n"
      "v_cmp_gt_u32 vcc, %[lb], v140\n"
      "s_cbranch_vccz Lx6%=\n"
      "s_sleep 1\n"
      "s_cmp_lg_u32 %[sp], 0\n"
      "s_cbranch_scc1 Lw6%=\n"
      "Lx6%=:\n"
      "ds_read_b128 v[100:103], v147 offset:0\n"
      "ds_read_b128 v[104:107], v147 offset:1024\n"
      "ds_read_b128 v[108:111], v147 offset:2048\n"
      "ds_read_b128 v[112:115], v147 offset:3072\n"
      "ds_read_b128 v[116:119], v147 offset:4096\n"
      "ds_read_b128 v[120:123], v147 offset:5120\n"
      "ds_read_b128 v[124:127], v147 offset:6144\n"
      "ds_read_b128 v[128:131], v147 offset:7168\n"
      "ds_read_b128 v[132:135], v147 offset:8192\n"
      "s_setprio 0\n"
      "ds_read_b128 v[136:139], v147 offset:9216\n"
      "s_waitcnt lgkmcnt(9)\n"
      "v_mfma_f32_16x16x32_bf16 v[0:3], v[100:103], v[60:63], v[0:3]\n"
      "ds_read_b128 v[100:103], v147 offset:10240\n"
      "s_waitcnt lgkmcnt(9)\n"
      "v_mfma_f32_16x16x32_bf16 v[4:7], v[104:107], v[60:63], v[4:7]\n"
      "ds_read_b128 v[104:107], v147 offset:11264\n"
      "s_waitcnt lgkmcnt(9)\n"
      "v_mfma_f32_16x16x32_bf16 v[8:11], v[108:111], v[60:63], v[8:11]\n"
      "ds_read_b128 v[108:111], v147 offset:12288\n"
      "s_waitcnt lgkmcnt(9)\n"
      "v_mfma_f32_16x16x32_bf16 v[12:15], v[112:115], v[60:63], v[12:15]\n"
      "ds_read_b128 v[112:115], v147 offset:13312\n"
      "s_waitcnt lgkmcnt(9)\n"
      "v_mfma_f32_16x16x32_bf16 v[16:19], v[116:119], v[60:63], v[16:19]\n"
      "ds_read_b128 v[116:119], v147 offset:14336\n"
      "s_waitcnt lgkmcnt(9)\n"
      "v_mfma_f32_16x16x32_bf16 v[0:3], v[120:123], v[64:67], v[0:3]\n"
      "ds_read_b128 v[120:123], v147 offset:15360\n"
      "s_waitcnt lgkmcnt(9)\n"
      "v_mfma_f32_16x16x32_bf16 v[4:7], v[124:127], v[64:67], v[4:7]\n"
      "ds_read_b128 v[124:127], v147 offset:16384\n"
      "s_waitcnt lgkmcnt(9)\n"
      "v_mfma_f32_16x16x32_bf16 v[8:11], v[128:131], v[64:67], v[8:11]\n"
      "ds_read_b128 v[128:131], v147 offset:17408\n"
      "s_waitcnt lgkmcnt(9)\n"
      "v_mfma_f32_16x16x32_bf16 v[12:15], v[132:135], v[64:67], v[12:15]\n"
      "ds_read_b128 v[132:135], v147 offset:18432\n"
      "s_waitcnt lgkmcnt(9)\n"
      "v_mfma_f32_16x16x32_bf16 v[16:19], v[136:139], v[64:67], v[16:19]\n"
      "ds_read_b128 v[136:139], v147 offset:19456\n"
      "s_waitcnt lgkmcnt(9)\n"
      "v_mfma_f32_16x16x32_bf16 v[0:3], v[100:103], v[68:71], v[0:3]\n"
      "ds_read_b128 v[100:103], v147 offset:20480\n"
      "s_waitcnt lgkmcnt(9)\n"
      "v_mfma_f32_16x16x32_bf16 v[4:7], v[104:107], v[68:71], v[4:7]\n"
      "ds_read_b128 v[104:107], v147 offset:21504\n"
      "s_waitcnt lgkmcnt(9)\n"
      "v_mfma_f32_16x16x32_bf16 v[8:11], v[108:111], v[68:71], v[8:11]\n"
      "ds_read_b128 v[108:111], v147 offset:22528\n"
      "s_waitcnt lgkmcnt(9)\n"
      "v_mfma_f32_16x16x32_bf16 v[12:15], v[112:115], v[68:71], v[12:15]\n"
      "ds_read_b128 v[112:115], v147 offset:23552\n"
      "s_waitcnt lgkmcnt(9)\n"
      "v_mfma_f32_16x16x32_bf16 v[16:19], v[116:119], v[68:71], v[16:19]\n"
      "ds_read_b128 v[116:119], v147 offset:24576\n"
      "s_waitcnt lgkmcnt(9)\n"
      "v_mfma_f32_16x16x32_bf16 v[0:3], v[120:123], v[72:75], v[0:3]\n"
      "ds_read_b128 v[120:123], v147 offset:25600\n"
      "s_waitcnt lgkmcnt(9)\n"
      "v_mfma_f32_16x16x32_bf16 v[4:7], v[124:127], v[72:75], v[4:7]\n"
      "ds_read_b128 v[124:127], v147 offset:26624\n"
      "s_waitcnt lgkmcnt(9)\n"
      "v_mfma_f32_16x16x32_bf16 v[8:11], v[128:131], v[72:75], v[8:11]\n"
      "ds_read_b128 v[128:131], v147 offset:27648\n"
      "s_waitcnt lgkmcnt(9)\n"
      "v_mfma_f32_16x16x32_bf16 v[12:15], v[132:135], v[72:75], v[12:15]\n"
      "ds_read_b128 v[132:135], v147 offset:28672\n"
      "s_waitcnt lgkmcnt(9)\n"
      "v_mfma_f32_16x16x32_bf16 v[16:19], v[136:139], v[72:75], v[16:19]\n"
      "ds_read_b128 v[136:139], v147 offset:29696\n"
      "s_waitcnt lgkmcnt(9)\n"
      "v_mfma_f32_16x16x32_bf16 v[0:3], v[100:103], v[76:79], v[0:3]\n"
      "ds_read_b128 v[100:103], v147 offset:30720\n"
      "s_waitcnt lgkmcnt(9)\n"
      "v_mfma_f32_16x16x32_bf16 v[4:7], v[104:107], v[76:79], v[4:7]\n"
      "ds_read_b128 v[104:107], v147 offset:31744\n"
      "s_waitcnt lgkmcnt(9)\n"
      "v_mfma_f32_16x16x32_bf16 v[8:11], v[108:111], v[76:79], v[8:11]\n"
      "ds_read_b128 v[108:111], v147 offset:32768\n"
      "s_waitcnt lgkmcnt(9)\n"
      "v_mfma_f32_16x16x32_bf16 v[12:15], v[112:115], v[76:79], v[12:15]\n"
      "ds_read_b128 v[112:115], v147 offset:33792\n"
      "s_waitcnt lgkmcnt(9)\n"
      "v_mfma_f32_16x16x32_bf16 v[16:19], v[116:119], v[76:79], v[16:19]\n"
      "ds_read_b128 v[116:119], v147 offset:34816\n"
      "s_waitcnt lgkmcnt(9)\n"
      "v_mfma_f32_16x16x32_bf16 v[0:3], v[120:123], v[80:83], v[0:3]\n"
      "ds_read_b128 v[120:123], v147 offset:35840\n"
      "s_waitcnt lgkmcnt(9)\n"
      "v_mfma_f32_16x16x32_bf16 v[4:7], v[124:127], v[80:83], v[4:7]\n"
      "ds_read_b128 v[124:127], v147 offset:36864\n"
      "s_waitcnt lgkmcnt(9)\n"
      "v_mfma_f32_16x16x32_bf16 v[8:11], v[128:131], v[80:83], v[8:11]\n"
      "ds_read_b128 v[128:131], v147 offset:37888\n"
      "s_waitcnt lgkmcnt(9)\n"
      "v_mfma_f32_16x16x32_bf16 v[12:15], v[132:135], v[80:83], v[12:15]\n"
      "ds_read_b128 v[132:135], v147 offset:38912\n"
      "s_waitcnt lgkmcnt(9)\n"
      "v_mfma_f32_16x16x32_bf16 v[16:19], v[136:139], v[80:83], v[16:19]\n"
      "ds_read_b128 v[136:139], v147 offset:39936\n"
      "s_waitcnt lgkmcnt(9)\n"
      "v_mfma_f32_16x16x32_bf16 v[0:3], v[100:103], v[84:87], v[0:3]\n"
      "ds_read_b128 v[100:103], v147 offset:40960\n"
      "s_waitcnt lgkmcnt(9)\n"
      "v_mfma_f32_16x16x32_bf16 v[4:7], v[104:107], v[84:87], v[4:7]\n"
      "ds_read_b128 v[104:107], v147 offset:41984\n"
      "s_waitcnt lgkmcnt(9)\n"
      "v_mfma_f32_16x16x32_bf16 v[8:11], v[108:111], v[84:87], v[8:11]\n"
      "ds_read_b128 v[108:111], v147 offset:43008\n"
      "s_waitcnt lgkmcnt(9)\n"
      "v_mfma_f32_16x16x32_bf16 v[12:15], v[112:115], v[84:87], v[12:15]\n"
      "ds_read_b128 v[112:115], v147 offset:44032\n"
      "s_waitcnt lgkmcnt(9)\n"
      "v_mfma_f32_16x16x32_bf16 v[16:19], v[116:119], v[84:87], v[16:19]\n"
      "ds_read_b128 v[116:119], v147 offset:45056\n"
      "s_waitcnt lgkmcnt(9)\n"
      "v_mfma_f32_16x16x32_bf16 v[0:3], v[120:123], v[88:91], v[0:3]\n"
      "ds_read_b128 v[120:123], v147 offset:46080\n"
      "s_waitcnt lgkmcnt(9)\n"
      "v_mfma_f32_16x16x32_bf16 v[4:7], v[124:127], v[88:91], v[4:7]\n"
      "ds_read_b128 v[124:127], v147 offset:47104\n"
      "s_waitcnt lgkmcnt(9)\n"
      "v_mfma_f32_16x16x32_bf16 v[8:11], v[128:131], v[88:91], v[8:11]\n"
      "ds_read_b128 v[128:131], v147 offset:48128\n"
      "s_waitcnt lgkmcnt(9)\n"
      "v_mfma_f32_16x16x32_bf16 v[12:15], v[132:135], v[88:91], v[12:15]\n"
      "ds_read_b128 v[132:135], v147 offset:49152\n"
      "s_waitcnt lgkmcnt(9)\n"
      "v_mfma_f32_16x16x32_bf16 v[16:19], v[136:139], v[88:91], v[16:19]\n"
      "ds_read_b128 v[136:139], v147 offset:50176\n"
      "s_waitcnt lgkmcnt(9)\n"
      "v_mfma_f32_16x16x32_bf16 v[0:3], v[100:103], v[92:95], v[0:3]\n"
      "s_waitcnt lgkmcnt(8)\n"
      "v_mfma_f32_16x16x32_bf16 v[4:7], v[104:107], v[92:95], v[4:7]\n"
      "s_waitcnt lgkmcnt(7)\n"
      "v_mfma_f32_16x16x32_bf16 v[8:11], v[108:111], v[92:95], v[8:11]\n"
      "s_waitcnt lgkmcnt(6)\n"
      "v_mfma_f32_16x16x32_bf16 v[12:15], v[112:115], v[92:95], v[12:15]\n"
      "s_waitcnt lgkmcnt(5)\n"
      "v_mfma_f32_16x16x32_bf16 v[16:19], v[116:119], v[92:95], v[16:19]\n"
      "s_waitcnt lgkmcnt(4)\n"
      "v_mfma_f32_16x16x32_bf16 v[0:3], v[120:123], v[96:99], v[0:3]\n"
      "s_waitcnt lgkmcnt(3)\n"
      "v_mfma_f32_16x16x32_bf16 v[4:7], v[124:127], v[96:99], v[4:7]\n"
      "s_waitcnt lgkmcnt(2)\n"
      "v_mfma_f32_16x16x32_bf16 v[8:11], v[128:131], v[96:99], v[8:11]\n"
      "s_waitcnt lgkmcnt(1)\n"
      "v_mfma_f32_16x16x32_bf16 v[12:15], v[132:135], v[96:99], v[12:15]\n"
      "s_waitcnt lgkmcnt(0)\n"
      "v_mfma_f32_16x16x32_bf16 v[16:19], v[136:139], v[96:99], v[16:19]\n"
      "s_setprio 2\n"
      "s_waitcnt lgkmcnt(0)\n"
      "s_mov_b64 exec, 1\n"
      "ds_add_u32 v142, v141 offset:0\n"
      "s_mov_b64 exec, -1\n"
      "s_add_u32 %[lb], %[lb], 4\n"
      "s_mov_b32 %[sp], %[cap]\n"
      "Lw7%=:\n"
      "ds_read_b32 v140, v142\n"
      "s_sub_u32 %[sp], %[sp], 1\n"
      "s_waitcnt lgkmcnt(0)\n"
      "v_cmp_gt_u32 vcc, %[lb], v140\n"
      "s_cbranch_vccz Lx7%=\n"
      "s_sleep 1\n"
      "s_cmp_lg_u32 %[sp], 0\n"
      "s_cbranch_scc1 Lw7%=\n"
      "Lx7%=:\n"
      "s_add_u32 s90, %[wl], 102400\n"
      "s_addc_u32 s91, %[wh], 0\n"
      "s_add_u32 m0, %[d0], 0\n"
      "s_nop 0\n"
      "global_load_lds_dwordx4 v144, s[90:91] offset:0 sc1\n"
      "global_load_lds_dwordx4 v144, s[90:91] offset:1024 sc1\n"
      "global_load_lds_dwordx4 v144, s[90:91] offset:2048 sc1\n"
      "global_load_lds_dwordx4 v144, s[90:91] offset:3072 sc1\n"
      "s_add_u32 s90, s90, 4096\n"
      "s_addc_u32 s91, s91, 0\n"
      "s_add_u32 m0, %[d0], 4096\n"
      "s_nop 0\n"
      "global_load_lds_dwordx4 v144, s[90:91] offset:0 sc1\n"
      "global_load_lds_dwordx4 v144, s[90:91] offset:1024 sc1\n"
      "global_load_lds_dwordx4 v144, s[90:91] offset:2048 sc1\n"
      "global_load_lds_dwordx4 v144, s[90:91] offset:3072 sc1\n"
      "s_add_u32 s90, s90, 4096\n"
      "s_addc_u32 s91, s91, 0\n"
      "s_add_u32 m0, %[d0], 8192\n"
      "s_nop 0\n"
      "global_load_lds_dwordx4 v144, s[90:91] offset:0 sc1\n"
      "global_load_lds_dwordx4 v144, s[90:91] offset:1024 sc1\n"
      "global_load_lds_dwordx4 v144, s[90:91] offset:2048 sc1\n"
      "global_load_lds_dwordx4 v144, s[90:91] offset:3072 sc1\n"
      "s_add_u32 s90, s90, 4096\n"
      "s_addc_u32 s91, s91, 0\n"
      "s_add_u32 m0, %[d0], 12288\n"
      "s_mov_b32 exec_hi, 0\n"
      "global_load_lds_dwordx4 v144, s[90:91] sc1\n"
      "s_mov_b32 exec_hi, -1\n"
      "s_cmp_eq_u32 s93, 1\n"
      "s_cbranch_scc1 Lq%=\n"
      "s_mov_b32 %[sp], %[cap]\n"
      "Lw8%=:\n"
      "ds_read_b32 v140, v148\n"
      "s_sub_u32 %[sp], %[sp], 1\n"
      "s_waitcnt lgkmcnt(0)\n"
      "v_cmp_gt_u32 vcc, s92, v140\n"
      "s_cbranch_vccz Lx8%=\n"
      "s_sleep 4\n"
      "s_cmp_lg_u32 %[sp], 0\n"
      "s_cbranch_scc1 Lw8%=\n"
      "Lx8%=:\n"
      "s_add_u32 s90, %[wl], 153600\n"
      "s_addc_u32 s91, %[wh], 0\n"
      "s_add_u32 m0, %[d1], 0\n"
      "s_nop 0\n"
      "global_load_lds_dwordx4 v144, s[90:91] offset:0 sc1\n"
      "global_load_lds_dwordx4 v144, s[90:91] offset:1024 sc1\n"
      "global_load_lds_dwordx4 v144, s[90:91] offset:2048 sc1\n"
      "global_load_lds_dwordx4 v144, s[90:91] offset:3072 sc1\n"
      "s_add_u32 s90, s90, 4096\n"
      "s_addc_u32 s91, s91, 0\n"
      "s_add_u32 m0, %[d1], 4096\n"
      "s_nop 0\n"
      "global_load_lds_dwordx4 v144, s[90:91] offset:0 sc1\n"
      "global_load_lds_dwordx4 v144, s[90:91] offset:1024 sc1\n"
      "global_load_lds_dwordx4 v144, s[90:91] offset:2048 sc1\n"
      "global_load_lds_dwordx4 v144, s[90:91] offset:3072 sc1\n"
      "s_add_u32 s90, s90, 4096\n"
      "s_addc_u32 s91, s91, 0\n"
      "s_add_u32 m0, %[d1], 8192\n"
      "s_nop 0\n"
      "global_load_lds_dwordx4 v144, s[90:91] offset:0 sc1\n"
      "global_load_lds_dwordx4 v144, s[90:91] offset:1024 sc1\n"
      "global_load_lds_dwordx4 v144, s[90:91] offset:2048 sc1\n"
      "global_load_lds_dwordx4 v144, s[90:91] offset:3072 sc1\n"
      "s_add_u32 s90, s90, 4096\n"
      "s_addc_u32 s91, s91, 0\n"
      "s_add_u32 m0, %[d1], 12288\n"
      "s_mov_b32 exec_hi, 0\n"
      "global_load_lds_dwordx4 v144, s[90:91] sc1\n"
      "s_mov_b32 exec_hi, -1\n"
      "ds_write_b32 v149, v150\n"
      "s_waitcnt lgkmcnt(0)\n"
      "s_mov_b64 exec, 1\n"
      "ds_add_u32 v142, v141 offset:8\n"
      "s_mov_b64 exec, -1\n"
      "Lq%=:\n"
      "s_waitcnt vmcnt(0)\n"
      "s_waitcnt lgkmcnt(0)\n"
      "s_mov_b64 exec, 1\n"
      "ds_add_u32 v142, v141 offset:4\n"
      "s_mov_b64 exec, -1\n"
      "s_add_u32 s92, %[bar], -64\n"
      "s_mov_b32 %[sp], %[cap]\n"
      "Lw9%=:\n"
      "ds_read_b32 v140, v148\n"
      "s_sub_u32 %[sp], %[sp], 1\n"
      "s_waitcnt lgkmcnt(0)\n"
      "v_cmp_gt_u32 vcc, s92, v140\n"
      "s_cbranch_vccz Lx9%=\n"
      "s_sleep 2\n"
      "s_cmp_lg_u32 %[sp], 0\n"
      "s_cbranch_scc1 Lw9%=\n"
      "Lx9%=:\n"
      "ds_write_b128 v151, v[0:3] offset:0\n"
      "ds_write_b128 v151, v[4:7] offset:1024\n"
      "ds_write_b128 v151, v[8:11] offset:2048\n"
      "ds_write_b128 v151, v[12:15] offset:3072\n"
      "ds_write_b128 v151, v[16:19] offset:4096\n"
      "v_mov_b32 v142, %[lp]\n"
      "s_waitcnt lgkmcnt(0)\n"
      "s_mov_b64 exec, 1\n"
      "ds_write_b32 v142, v141\n"
      "s_mov_b64 exec, -1\n"
        :
          [sp] "=&s"(sp), [lb] "+s"(lb)
        : "{v[144:151]}"(in), [src] "s"(src), [sr2] "s"(src + 8192), [wl] "s"((unsigned)wsrc), [wh] "s"((unsigned)(wsrc >> 32)), [d0] "s"(lds0 + L_W0 + lw * 12800), [d1] "s"(lds0 + L_W1 + lw * 12800),
          [bar] "s"(lds0 + SY_LBAR), [lp] "s"(lds0 + SY_LPROG + lw * 4), [cap] "s"(LSPIN)
        : "v0", "v1", "v2", "v3", "v4", "v5", "v6", "v7", "v8", "v9", "v10", "v11", "v12", "v13", "v14", "v15", "v16", "v17", "v18", "v19", "v20", "v21", "v22", "v23", "v24", "v25", "v26", "v27", "v28", "v29", "v30", "v31", "v32", "v33", "v34", "v35", "v36", "v37", "v38", "v39", "v40", "v41", "v42", "v43", "v44", "v45", "v46", "v47", "v48", "v49", "v50", "v51", "v52", "v53", "v54", "v55", "v56", "v57", "v58", "v59", "v60", "v61", "v62", "v63", "v64", "v65", "v66", "v67", "v68", "v69", "v70", "v71", "v72", "v73", "v74", "v75", "v76", "v77", "v78", "v79", "v80", "v81", "v82", "v83", "v84", "v85", "v86", "v87", "v88", "v89", "v90", "v91", "v92", "v93", "v94", "v95", "v96", "v97", "v98", "v99", "v100", "v101", "v102", "v103", "v104", "v105", "v106", "v107", "v108", "v109", "v110", "v111", "v112", "v113", "v114", "v115", "v116", "v117", "v118", "v119", "v120", "v121", "v122", "v123", "v124", "v125", "v126", "v127", "v128", "v129", "v130", "v131", "v132", "v133", "v134", "v135", "v136", "v137", "v138", "v139", "v140", "v141", "v142", "v143", "s76", "s77", "s78", "s79", "s80", "s81", "s82", "s83", "s84", "s85", "s90", "s91", "s92", "s93", "vcc", "scc", "memory");
    }
    asm volatile("Lfused_end:");
    return;
  }

  const int qp = wid & 3, kh = wid >> 2;
  const int own = qb * 8 + qp * 2 + kh;
  const unsigned sy = lds0 + L_SY, pgm = lds0 + SY_PROG + wid * 4, pgp = lds0 + SY_PROG + (wid ^ 4) * 4;
  char* myflag;
  u32x16 at;
  f32x16 o0, o1;
  u32x2 z0;
  u32x4 z1;
  {
    LANES;
    const int kb = L_S0 + kh * 4 * TKV;
    const unsigned qs = lds0 + L_Q + (qp * 2 + 1 - kh) * TQ + posq, qt = lds0 + L_Q + (qp * 2 + kh) * TQ + posq, zr = lds0 + kb + TQ + 336;
    const unsigned rec0 = lds0 + L_MRG0 + qp * 3072 + lane * 16, rec1 = lds0 + L_MRG1 + qp * 5376 + lane * 16, recm = kh ? rec1 : rec0, recp = kh ? rec0 : rec1;
    const unsigned ot = (own * 2 + (r >> 4)) * 20480;
    at = u32x16{0u, lds0 + kb + h * 512 + posk, lds0 + kb + (h ? TQ + 320 : 1024 + posk), lds0 + kb + TQ + h * VB + vslot * 16,
                qs + h * 512, qt + h * 512, h ? zr : qs + 1024, h ? zr : qt + 1024,
                recm, recp, recp + 2048 + (1 - h) * 512, lds0 + L_G + ((qp * 2 + kh) * 32 + r) * 4, lds0 + SY_PROG + (lane < 8 ? lane : 10) * 4, lane < 8 ? sy + 28 : 4u,
                ot + (head * 2 + h) * 256 + l16 * 16, ot + 16384 + head * 128 + l16 * 8};
  }
  if (idx < 30) {
    LANES;
    const unsigned tl = lds0 + L_Q + 12288 + wid * 256;
    myflag = ws + WS_FA + (col * 4 + (wid & 3)) * 256 + (row * 2 + (wid >> 2)) * 8;
    const int wr = wid >> 2, wc = wid & 3;
    const int xr = wr * 64 + l16, q4 = l16 >> 2, sz = q4 | ((g4 & 1) << 2);
    const unsigned swz = (xr >> 1) & 7;
    const unsigned wb = lds0 + (8 * g4 + q4) * 256 + (lane & 3) * 8, xa = lds0 + L_XS + xr * 128;
    u32x4 ad = {xa + ((g4 ^ swz) * 16), xa + (((4 + g4) ^ swz) * 16), wb + (((wc * 2) ^ sz) * 32), wb + (((wc * 2 + 1) ^ sz) * 32)};
    const int gcol = col * 128 + wc * 32, type = gcol / 640, hcol = gcol - type * 640;
    const int T0 = row * 4 + wr * 2, ts = type == 0 ? TQ : TS;
    u32x2 so;
#pragma unroll
    for (int n = 0; n < 2; ++n) {
      if (type == 2) {
        so[n] = T0 * TS + TS_V + g4 * 10240 + (hcol + 16 * n + l16) * 16;
      } else {
        const int qd = (hcol >> 2) + 4 * n + g4, hd = qd / 5, c = qd - hd * 5;
        so[n] = T0 * ts + hd * (type == 0 ? 64 * TQ : TQ) + c * 256 + l16 * 16;
      }
    }
    const char* sb = ws + (type == 0 ? WS_QB : WS_KVB);
    const char* fb = myflag - (WS_FA - WS_FB);
    const char* sa = ws + (type == 0 ? WS_QA : WS_KVA);
    u64 xt = XT1;
    unsigned sc = type == 0 ? __builtin_bit_cast(unsigned, QSCALE) : 0x3F800000u;
    int sl, st, cnt, tmp;
    asm volatile(
      "s_mov_b32 %[cnt], 10\n"
      "s_mov_b32 %[st], 0\n"
      "s_mov_b32 %[sl], 0\n"
      "s_mov_b64 vcc, -1\n"
      "s_cmp_eq_u32 %[ty], 2\n"
      "s_cbranch_scc1 LV%=\n"
      "s_barrier\n"
      "ds_read_b64_tr_b16 v[64:65], v162 offset:0\n"
      "ds_read_b64_tr_b16 v[66:67], v162 offset:1024\n"
      "ds_read_b128 v[48:51], v160 offset:0\n"
      "ds_read_b128 v[52:55], v160 offset:2048\n"
      "ds_read_b128 v[56:59], v160 offset:4096\n"
      "ds_read_b128 v[60:63], v160 offset:6144\n"
      "ds_read_b64_tr_b16 v[68:69], v163 offset:0\n"
      "ds_read_b64_tr_b16 v[70:71], v163 offset:1024\n"
      "ds_read_b64_tr_b16 v[88:89], v162 offset:8192\n"
      "ds_read_b64_tr_b16 v[90:91], v162 offset:9216\n"
      "ds_read_b128 v[72:75], v161 offset:0\n"
      "ds_read_b128 v[76:79], v161 offset:2048\n"
      "ds_read_b128 v[80:83], v161 offset:4096\n"
      "ds_read_b128 v[84:87], v161 offset:6144\n"
      "ds_read_b64_tr_b16 v[92:93], v163 offset:8192\n"
      "ds_read_b64_tr_b16 v[94:95], v163 offset:9216\n"
      "s_cmp_eq_u32 %[sl], 2\n"
      "s_cselect_b32 %[tmp], -2, 1\n"
      "s_add_i32 %[sl], %[sl], %[tmp]\n"
      "s_lshl_b32 %[tmp], %[tmp], 14\n"
      "s_bfe_i64 s[98:99], %[xt], 0x40000\n"
      "s_lshr_b64 %[xt], %[xt], 4\n"
      "s_lshl_b32 s98, s98, 14\n"
      "v_add_u32 v160, s98, v160\n"
      "v_add_u32 v161, s98, v161\n"
      "v_add_u32 v162, %[tmp], v162\n"
      "v_add_u32 v163, %[tmp], v163\n"
      "s_waitcnt lgkmcnt(0)\n"
      "Lg%=:\n"
      "s_barrier\n"
      "s_cbranch_vccnz Lf%=\n"
      "v_mfma_f32_16x16x32_bf16 v[16:19], v[64:67], v[48:51], v[16:19]\n"
      "ds_read_b64_tr_b16 v[112:113], v162 offset:0\n"
      "v_mfma_f32_16x16x32_bf16 v[20:23], v[64:67], v[52:55], v[20:23]\n"
      "ds_read_b64_tr_b16 v[114:115], v162 offset:1024\n"
      "v_mfma_f32_16x16x32_bf16 v[24:27], v[64:67], v[56:59], v[24:27]\n"
      "ds_read_b128 v[96:99], v160 offset:0\n"
      "v_mfma_f32_16x16x32_bf16 v[28:31], v[64:67], v[60:63], v[28:31]\n"
      "ds_read_b128 v[100:103], v160 offset:2048\n"
      "v_mfma_f32_16x16x32_bf16 v[32:35], v[68:71], v[48:51], v[32:35]\n"
      "ds_read_b128 v[104:107], v160 offset:4096\n"
      "v_mfma_f32_16x16x32_bf16 v[36:39], v[68:71], v[52:55], v[36:39]\n"
      "ds_read_b128 v[108:111], v160 offset:6144\n"
      "v_mfma_f32_16x16x32_bf16 v[40:43], v[68:71], v[56:59], v[40:43]\n"
      "ds_read_b64_tr_b16 v[116:117], v163 offset:0\n"
      "v_mfma_f32_16x16x32_bf16 v[44:47], v[68:71], v[60:63], v[44:47]\n"
      "ds_read_b64_tr_b16 v[118:119], v163 offset:1024\n"
      "Lr%=:\n"
      "v_mfma_f32_16x16x32_bf16 v[16:19], v[88:91], v[72:75], v[16:19]\n"
      "ds_read_b64_tr_b16 v[136:137], v162 offset:8192\n"
      "v_mfma_f32_16x16x32_bf16 v[20:23], v[88:91], v[76:79], v[20:23]\n"
      "ds_read_b64_tr_b16 v[138:139], v162 offset:9216\n"
      "s_cmp_eq_u32 %[sl], 2\n"
      "s_cselect_b32 %[tmp], -2, 1\n"
      "s_add_i32 %[sl], %[sl], %[tmp]\n"
      "s_lshl_b32 %[tmp], %[tmp], 14\n"
      "s_bfe_i64 s[98:99], %[xt], 0x40000\n"
      "s_lshr_b64 %[xt], %[xt], 4\n"
      "s_lshl_b32 s98, s98, 14\n"
      "v_mfma_f32_16x16x32_bf16 v[24:27], v[88:91], v[80:83], v[24:27]\n"
      "ds_read_b128 v[120:123], v161 offset:0\n"
      "v_mfma_f32_16x16x32_bf16 v[28:31], v[88:91], v[84:87], v[28:31]\n"
      "ds_read_b128 v[124:127], v161 offset:2048\n"
      "v_mfma_f32_16x16x32_bf16 v[32:35], v[92:95], v[72:75], v[32:35]\n"
      "ds_read_b128 v[128:131], v161 offset:4096\n"
      "v_mfma_f32_16x16x32_bf16 v[36:39], v[92:95], v[76:79], v[36:39]\n"
      "ds_read_b128 v[132:135], v161 offset:6144\n"
      "v_add_u32 v160, s98, v160\n"
      "v_add_u32 v161, s98, v161\n"
      "v_mfma_f32_16x16x32_bf16 v[40:43], v[92:95], v[80:83], v[40:43]\n"
      "ds_read_b64_tr_b16 v[140:141], v163 offset:8192\n"
      "v_mfma_f32_16x16x32_bf16 v[44:47], v[92:95], v[84:87], v[44:47]\n"
      "ds_read_b64_tr_b16 v[142:143], v163 offset:9216\n"
      "v_add_u32 v162, %[tmp], v162\n"
      "v_add_u32 v163, %[tmp], v163\n"
      "s_cmp_eq_u32 %[st], 0\n"
      "s_cbranch_scc0 Lh%=\n"
      "Li%=:\n"
      "s_cmp_eq_u32 %[cnt], 1\n"
      "s_waitcnt lgkmcnt(0)\n"
      "s_cbranch_scc1 Ln%=\n"
      "s_barrier\n"
      "Ln%=:\n"
      "v_mfma_f32_16x16x32_bf16 v[16:19], v[112:115], v[96:99], v[16:19]\n"
      "ds_read_b64_tr_b16 v[64:65], v162 offset:0\n"
      "v_mfma_f32_16x16x32_bf16 v[20:23], v[112:115], v[100:103], v[20:23]\n"
      "ds_read_b64_tr_b16 v[66:67], v162 offset:1024\n"
      "v_mfma_f32_16x16x32_bf16 v[24:27], v[112:115], v[104:107], v[24:27]\n"
      "ds_read_b128 v[48:51], v160 offset:0\n"
      "v_mfma_f32_16x16x32_bf16 v[28:31], v[112:115], v[108:111], v[28:31]\n"
      "ds_read_b128 v[52:55], v160 offset:2048\n"
      "v_mfma_f32_16x16x32_bf16 v[32:35], v[116:119], v[96:99], v[32:35]\n"
      "ds_read_b128 v[56:59], v160 offset:4096\n"
      "v_mfma_f32_16x16x32_bf16 v[36:39], v[116:119], v[100:103], v[36:39]\n"
      "ds_read_b128 v[60:63], v160 offset:6144\n"
      "v_mfma_f32_16x16x32_bf16 v[40:43], v[116:119], v[104:107], v[40:43]\n"
      "ds_read_b64_tr_b16 v[68:69], v163 offset:0\n"
      "v_mfma_f32_16x16x32_bf16 v[44:47], v[116:119], v[108:111], v[44:47]\n"
      "ds_read_b64_tr_b16 v[70:71], v163 offset:1024\n"
      "v_mfma_f32_16x16x32_bf16 v[16:19], v[136:139], v[120:123], v[16:19]\n"
      "ds_read_b64_tr_b16 v[88:89], v162 offset:8192\n"
      "v_mfma_f32_16x16x32_bf16 v[20:23], v[136:139], v[124:127], v[20:23]\n"
      "ds_read_b64_tr_b16 v[90:91], v162 offset:9216\n"
      "s_cmp_eq_u32 %[sl], 2\n"
      "s_cselect_b32 %[tmp], -2, 1\n"
      "s_add_i32 %[sl], %[sl], %[tmp]\n"
      "s_lshl_b32 %[tmp], %[tmp], 14\n"
      "s_bfe_i64 s[98:99], %[xt], 0x40000\n"
      "s_lshr_b64 %[xt], %[xt], 4\n"
      "s_lshl_b32 s98, s98, 14\n"
      "v_mfma_f32_16x16x32_bf16 v[24:27], v[136:139], v[128:131], v[24:27]\n"
      "ds_read_b128 v[72:75], v161 offset:0\n"
      "v_mfma_f32_16x16x32_bf16 v[28:31], v[136:139], v[132:135], v[28:31]\n"
      "ds_read_b128 v[76:79], v161 offset:2048\n"
      "v_mfma_f32_16x16x32_bf16 v[32:35], v[140:143], v[120:123], v[32:35]\n"
      "ds_read_b128 v[80:83], v161 offset:4096\n"
      "v_mfma_f32_16x16x32_bf16 v[36:39], v[140:143], v[124:127], v[36:39]\n"
      "ds_read_b128 v[84:87], v161 offset:6144\n"
      "v_add_u32 v160, s98, v160\n"
      "v_add_u32 v161, s98, v161\n"
      "v_mfma_f32_16x16x32_bf16 v[40:43], v[140:143], v[128:131], v[40:43]\n"
      "ds_read_b64_tr_b16 v[92:93], v163 offset:8192\n"
      "v_mfma_f32_16x16x32_bf16 v[44:47], v[140:143], v[132:135], v[44:47]\n"
      "ds_read_b64_tr_b16 v[94:95], v163 offset:9216\n"
      "v_add_u32 v162, %[tmp], v162\n"
      "v_add_u32 v163, %[tmp], v163\n"
      "s_cmp_eq_u32 %[st], 0\n"
      "s_cbranch_scc0 Lj%=\n"
      "Lk%=:\n"
      "s_sub_i32 %[cnt], %[cnt], 1\n"
      "s_cmp_eq_u32 %[cnt], 5\n"
      "s_cbranch_scc1 Lp%=\n"
      "s_cmp_lg_u32 %[cnt], 0\n"
      "s_waitcnt lgkmcnt(0)\n"
      "s_cbranch_scc1 Lg%=\n"
      "Lp%=:\n"
      "s_nop 3\n"
      "s_cmp_eq_u32 %[sc], 1.0\n"
      "s_cbranch_scc1 Lq%=\n"
      "v_mul_f32 v16, %[sc], v16\n"
      "v_mul_f32 v17, %[sc], v17\n"
      "v_mul_f32 v18, %[sc], v18\n"
      "v_mul_f32 v19, %[sc], v19\n"
      "v_mul_f32 v20, %[sc], v20\n"
      "v_mul_f32 v21, %[sc], v21\n"
      "v_mul_f32 v22, %[sc], v22\n"
      "v_mul_f32 v23, %[sc], v23\n"
      "v_mul_f32 v24, %[sc], v24\n"
      "v_mul_f32 v25, %[sc], v25\n"
      "v_mul_f32 v26, %[sc], v26\n"
      "v_mul_f32 v27, %[sc], v27\n"
      "v_mul_f32 v28, %[sc], v28\n"
      "v_mul_f32 v29, %[sc], v29\n"
      "v_mul_f32 v30, %[sc], v30\n"
      "v_mul_f32 v31, %[sc], v31\n"
      "v_mul_f32 v32, %[sc], v32\n"
      "v_mul_f32 v33, %[sc], v33\n"
      "v_mul_f32 v34, %[sc], v34\n"
      "v_mul_f32 v35, %[sc], v35\n"
      "v_mul_f32 v36, %[sc], v36\n"
      "v_mul_f32 v37, %[sc], v37\n"
      "v_mul_f32 v38, %[sc], v38\n"
      "v_mul_f32 v39, %[sc], v39\n"
      "v_mul_f32 v40, %[sc], v40\n"
      "v_mul_f32 v41, %[sc], v41\n"
      "v_mul_f32 v42, %[sc], v42\n"
      "v_mul_f32 v43, %[sc], v43\n"
      "v_mul_f32 v44, %[sc], v44\n"
      "v_mul_f32 v45, %[sc], v45\n"
      "v_mul_f32 v46, %[sc], v46\n"
      "v_mul_f32 v47, %[sc], v47\n"
      "s_mov_b32 %[sc], 1.0\n"
      "Lq%=:\n"
      "v_cvt_pk_bf16_f32 v144, v16, v17\n"
      "v_cvt_pk_bf16_f32 v145, v18, v19\n"
      "v_cvt_pk_bf16_f32 v146, v20, v21\n"
      "v_cvt_pk_bf16_f32 v147, v22, v23\n"
      "v_cvt_pk_bf16_f32 v148, v24, v25\n"
      "v_cvt_pk_bf16_f32 v149, v26, v27\n"
      "v_cvt_pk_bf16_f32 v150, v28, v29\n"
      "v_cvt_pk_bf16_f32 v151, v30, v31\n"
      "v_cvt_pk_bf16_f32 v152, v32, v33\n"
      "v_cvt_pk_bf16_f32 v153, v34, v35\n"
      "v_cvt_pk_bf16_f32 v154, v36, v37\n"
      "v_cvt_pk_bf16_f32 v155, v38, v39\n"
      "v_cvt_pk_bf16_f32 v156, v40, v41\n"
      "v_cvt_pk_bf16_f32 v157, v42, v43\n"
      "v_cvt_pk_bf16_f32 v158, v44, v45\n"
      "v_cvt_pk_bf16_f32 v159, v46, v47\n"
      "s_cmp_eq_u32 %[cnt], 0\n"
      "s_cbranch_scc1 Lz%=\n"
      "s_mov_b64 %[xt], %[xt2]\n"
      "s_mov_b64 vcc, -1\n"
      "s_mov_b32 %[st], 2\n"
      "s_waitcnt vmcnt(0) lgkmcnt(0)\n"
      "global_store_dwordx4 v164, v[144:147], %[sb0] sc1\n"
      "global_store_dwordx4 v164, v[148:151], %[sb1] sc1\n"
      "s_branch Lg%=\n"
      "Lf%=:\n"
      "s_mov_b64 vcc, 0\n"
      "v_mfma_f32_16x16x32_bf16 v[16:19], v[64:67], v[48:51], 0\n"
      "ds_read_b64_tr_b16 v[112:113], v162 offset:0\n"
      "v_mfma_f32_16x16x32_bf16 v[20:23], v[64:67], v[52:55], 0\n"
      "ds_read_b64_tr_b16 v[114:115], v162 offset:1024\n"
      "v_mfma_f32_16x16x32_bf16 v[24:27], v[64:67], v[56:59], 0\n"
      "ds_read_b128 v[96:99], v160 offset:0\n"
      "v_mfma_f32_16x16x32_bf16 v[28:31], v[64:67], v[60:63], 0\n"
      "ds_read_b128 v[100:103], v160 offset:2048\n"
      "v_mfma_f32_16x16x32_bf16 v[32:35], v[68:71], v[48:51], 0\n"
      "ds_read_b128 v[104:107], v160 offset:4096\n"
      "v_mfma_f32_16x16x32_bf16 v[36:39], v[68:71], v[52:55], 0\n"
      "ds_read_b128 v[108:111], v160 offset:6144\n"
      "v_mfma_f32_16x16x32_bf16 v[40:43], v[68:71], v[56:59], 0\n"
      "ds_read_b64_tr_b16 v[116:117], v163 offset:0\n"
      "v_mfma_f32_16x16x32_bf16 v[44:47], v[68:71], v[60:63], 0\n"
      "ds_read_b64_tr_b16 v[118:119], v163 offset:1024\n"
      "s_branch Lr%=\n"
      "Lh%=:\n"
      "s_cmp_lg_u32 %[st], 2\n"
      "s_cbranch_scc1 Lh1%=\n"
      "global_store_dwordx4 v165, v[152:155], %[sb0] sc1\n"
      "global_store_dwordx4 v165, v[156:159], %[sb1] sc1\n"
      "s_mov_b32 %[st], 1\n"
      "s_branch Li%=\n"
      "Lh1%=:\n"
      "s_getreg_b32 %[tmp], hwreg(HW_REG_IB_STS, 0, 4)\n"
      "s_cmp_le_u32 %[cnt], 3\n"
      "s_cselect_b32 %[tmp], 0, %[tmp]\n"
      "s_cmp_lg_u32 %[tmp], 0\n"
      "s_cbranch_scc1 Li%=\n"
      "v_mov_b32 v146, %[ta]\n"
      "ds_read_b64 v[144:145], v146\n"
      "v_mov_b32 v147, 0\n"
      "s_waitcnt vmcnt(0) lgkmcnt(0)\n"
      "s_mov_b64 exec, 1\n"
      "global_store_dwordx2 v147, v[144:145], %[fl] sc1\n"
      "s_mov_b64 exec, -1\n"
      "s_mov_b32 %[st], 0\n"
      "s_branch Li%=\n"
      "Lj%=:\n"
      "s_getreg_b32 %[tmp], hwreg(HW_REG_IB_STS, 0, 4)\n"
      "s_cmp_le_u32 %[cnt], 3\n"
      "s_cselect_b32 %[tmp], 0, %[tmp]\n"
      "s_cmp_lg_u32 %[tmp], 0\n"
      "s_cbranch_scc1 Lk%=\n"
      "v_mov_b32 v146, %[ta]\n"
      "ds_read_b64 v[144:145], v146\n"
      "v_mov_b32 v147, 0\n"
      "s_waitcnt vmcnt(0) lgkmcnt(0)\n"
      "s_mov_b64 exec, 1\n"
      "global_store_dwordx2 v147, v[144:145], %[fl] sc1\n"
      "s_mov_b64 exec, -1\n"
      "s_mov_b32 %[st], 0\n"
      "s_branch Lk%=\n"
      "LV%=:\n"
      "s_barrier\n"
      "ds_read_b64_tr_b16 v[64:65], v162 offset:0\n"
      "ds_read_b64_tr_b16 v[66:67], v162 offset:1024\n"
      "ds_read_b128 v[48:51], v160 offset:0\n"
      "ds_read_b128 v[52:55], v160 offset:2048\n"
      "ds_read_b128 v[56:59], v160 offset:4096\n"
      "ds_read_b128 v[60:63], v160 offset:6144\n"
      "ds_read_b64_tr_b16 v[68:69], v163 offset:0\n"
      "ds_read_b64_tr_b16 v[70:71], v163 offset:1024\n"
      "ds_read_b64_tr_b16 v[88:89], v162 offset:8192\n"
      "ds_read_b64_tr_b16 v[90:91], v162 offset:9216\n"
      "ds_read_b128 v[72:75], v161 offset:0\n"
      "ds_read_b128 v[76:79], v161 offset:2048\n"
      "ds_read_b128 v[80:83], v161 offset:4096\n"
      "ds_read_b128 v[84:87], v161 offset:6144\n"
      "ds_read_b64_tr_b16 v[92:93], v163 offset:8192\n"
      "ds_read_b64_tr_b16 v[94:95], v163 offset:9216\n"
      "s_cmp_eq_u32 %[sl], 2\n"
      "s_cselect_b32 %[tmp], -2, 1\n"
      "s_add_i32 %[sl], %[sl], %[tmp]\n"
      "s_lshl_b32 %[tmp], %[tmp], 14\n"
      "s_bfe_i64 s[98:99], %[xt], 0x40000\n"
      "s_lshr_b64 %[xt], %[xt], 4\n"
      "s_lshl_b32 s98, s98, 14\n"
      "v_add_u32 v160, s98, v160\n"
      "v_add_u32 v161, s98, v161\n"
      "v_add_u32 v162, %[tmp], v162\n"
      "v_add_u32 v163, %[tmp], v163\n"
      "s_waitcnt lgkmcnt(0)\n"
      "Lgv%=:\n"
      "s_barrier\n"
      "s_cbranch_vccnz Lfv%=\n"
      "v_mfma_f32_16x16x32_bf16 v[16:19], v[48:51], v[64:67], v[16:19]\n"
      "ds_read_b64_tr_b16 v[112:113], v162 offset:0\n"
      "v_mfma_f32_16x16x32_bf16 v[20:23], v[52:55], v[64:67], v[20:23]\n"
      "ds_read_b64_tr_b16 v[114:115], v162 offset:1024\n"
      "v_mfma_f32_16x16x32_bf16 v[24:27], v[56:59], v[64:67], v[24:27]\n"
      "ds_read_b128 v[96:99], v160 offset:0\n"
      "v_mfma_f32_16x16x32_bf16 v[28:31], v[60:63], v[64:67], v[28:31]\n"
      "ds_read_b128 v[100:103], v160 offset:2048\n"
      "v_mfma_f32_16x16x32_bf16 v[32:35], v[48:51], v[68:71], v[32:35]\n"
      "ds_read_b128 v[104:107], v160 offset:4096\n"
      "v_mfma_f32_16x16x32_bf16 v[36:39], v[52:55], v[68:71], v[36:39]\n"
      "ds_read_b128 v[108:111], v160 offset:6144\n"
      "v_mfma_f32_16x16x32_bf16 v[40:43], v[56:59], v[68:71], v[40:43]\n"
      "ds_read_b64_tr_b16 v[116:117], v163 offset:0\n"
      "v_mfma_f32_16x16x32_bf16 v[44:47], v[60:63], v[68:71], v[44:47]\n"
      "ds_read_b64_tr_b16 v[118:119], v163 offset:1024\n"
      "Lrv%=:\n"
      "v_mfma_f32_16x16x32_bf16 v[16:19], v[72:75], v[88:91], v[16:19]\n"
      "ds_read_b64_tr_b16 v[136:137], v162 offset:8192\n"
      "v_mfma_f32_16x16x32_bf16 v[20:23], v[76:79], v[88:91], v[20:23]\n"
      "ds_read_b64_tr_b16 v[138:139], v162 offset:9216\n"
      "s_cmp_eq_u32 %[sl], 2\n"
      "s_cselect_b32 %[tmp], -2, 1\n"
      "s_add_i32 %[sl], %[sl], %[tmp]\n"
      "s_lshl_b32 %[tmp], %[tmp], 14\n"
      "s_bfe_i64 s[98:99], %[xt], 0x40000\n"
      "s_lshr_b64 %[xt], %[xt], 4\n"
      "s_lshl_b32 s98, s98, 14\n"
      "v_mfma_f32_16x16x32_bf16 v[24:27], v[80:83], v[88:91], v[24:27]\n"
      "ds_read_b128 v[120:123], v161 offset:0\n"
      "v_mfma_f32_16x16x32_bf16 v[28:31], v[84:87], v[88:91], v[28:31]\n"
      "ds_read_b128 v[124:127], v161 offset:2048\n"
      "v_mfma_f32_16x16x32_bf16 v[32:35], v[72:75], v[92:95], v[32:35]\n"
      "ds_read_b128 v[128:131], v161 offset:4096\n"
      "v_mfma_f32_16x16x32_bf16 v[36:39], v[76:79], v[92:95], v[36:39]\n"
      "ds_read_b128 v[132:135], v161 offset:6144\n"
      "v_add_u32 v160, s98, v160\n"
      "v_add_u32 v161, s98, v161\n"
      "v_mfma_f32_16x16x32_bf16 v[40:43], v[80:83], v[92:95], v[40:43]\n"
      "ds_read_b64_tr_b16 v[140:141], v163 offset:8192\n"
      "v_mfma_f32_16x16x32_bf16 v[44:47], v[84:87], v[92:95], v[44:47]\n"
      "ds_read_b64_tr_b16 v[142:143], v163 offset:9216\n"
      "v_add_u32 v162, %[tmp], v162\n"
      "v_add_u32 v163, %[tmp], v163\n"
      "s_cmp_eq_u32 %[st], 0\n"
      "s_cbranch_scc0 Lhv%=\n"
      "Liv%=:\n"
      "s_cmp_eq_u32 %[cnt], 1\n"
      "s_waitcnt lgkmcnt(0)\n"
      "s_cbranch_scc1 Lnv%=\n"
      "s_barrier\n"
      "Lnv%=:\n"
      "v_mfma_f32_16x16x32_bf16 v[16:19], v[96:99], v[112:115], v[16:19]\n"
      "ds_read_b64_tr_b16 v[64:65], v162 offset:0\n"
      "v_mfma_f32_16x16x32_bf16 v[20:23], v[100:103], v[112:115], v[20:23]\n"
      "ds_read_b64_tr_b16 v[66:67], v162 offset:1024\n"
      "v_mfma_f32_16x16x32_bf16 v[24:27], v[104:107], v[112:115], v[24:27]\n"
      "ds_read_b128 v[48:51], v160 offset:0\n"
      "v_mfma_f32_16x16x32_bf16 v[28:31], v[108:111], v[112:115], v[28:31]\n"
      "ds_read_b128 v[52:55], v160 offset:2048\n"
      "v_mfma_f32_16x16x32_bf16 v[32:35], v[96:99], v[116:119], v[32:35]\n"
      "ds_read_b128 v[56:59], v160 offset:4096\n"
      "v_mfma_f32_16x16x32_bf16 v[36:39], v[100:103], v[116:119], v[36:39]\n"
      "ds_read_b128 v[60:63], v160 offset:6144\n"
      "v_mfma_f32_16x16x32_bf16 v[40:43], v[104:107], v[116:119], v[40:43]\n"
      "ds_read_b64_tr_b16 v[68:69], v163 offset:0\n"
      "v_mfma_f32_16x16x32_bf16 v[44:47], v[108:111], v[116:119], v[44:47]\n"
      "ds_read_b64_tr_b16 v[70:71], v163 offset:1024\n"
      "v_mfma_f32_16x16x32_bf16 v[16:19], v[120:123], v[136:139], v[16:19]\n"
      "ds_read_b64_tr_b16 v[88:89], v162 offset:8192\n"
      "v_mfma_f32_16x16x32_bf16 v[20:23], v[124:127], v[136:139], v[20:23]\n"
      "ds_read_b64_tr_b16 v[90:91], v162 offset:9216\n"
      "s_cmp_eq_u32 %[sl], 2\n"
      "s_cselect_b32 %[tmp], -2, 1\n"
      "s_add_i32 %[sl], %[sl], %[tmp]\n"
      "s_lshl_b32 %[tmp], %[tmp], 14\n"
      "s_bfe_i64 s[98:99], %[xt], 0x40000\n"
      "s_lshr_b64 %[xt], %[xt], 4\n"
      "s_lshl_b32 s98, s98, 14\n"
      "v_mfma_f32_16x16x32_bf16 v[24:27], v[128:131], v[136:139], v[24:27]\n"
      "ds_read_b128 v[72:75], v161 offset:0\n"
      "v_mfma_f32_16x16x32_bf16 v[28:31], v[132:135], v[136:139], v[28:31]\n"
      "ds_read_b128 v[76:79], v161 offset:2048\n"
      "v_mfma_f32_16x16x32_bf16 v[32:35], v[120:123], v[140:143], v[32:35]\n"
      "ds_read_b128 v[80:83], v161 offset:4096\n"
      "v_mfma_f32_16x16x32_bf16 v[36:39], v[124:127], v[140:143], v[36:39]\n"
      "ds_read_b128 v[84:87], v161 offset:6144\n"
      "v_add_u32 v160, s98, v160\n"
      "v_add_u32 v161, s98, v161\n"
      "v_mfma_f32_16x16x32_bf16 v[40:43], v[128:131], v[140:143], v[40:43]\n"
      "ds_read_b64_tr_b16 v[92:93], v163 offset:8192\n"
      "v_mfma_f32_16x16x32_bf16 v[44:47], v[132:135], v[140:143], v[44:47]\n"
      "ds_read_b64_tr_b16 v[94:95], v163 offset:9216\n"
      "v_add_u32 v162, %[tmp], v162\n"
      "v_add_u32 v163, %[tmp], v163\n"
      "s_cmp_eq_u32 %[st], 0\n"
      "s_cbranch_scc0 Ljv%=\n"
      "Lkv%=:\n"
      "s_sub_i32 %[cnt], %[cnt], 1\n"
      "s_cmp_eq_u32 %[cnt], 5\n"
      "s_cbranch_scc1 Lpv%=\n"
      "s_cmp_lg_u32 %[cnt], 0\n"
      "s_waitcnt lgkmcnt(0)\n"
      "s_cbranch_scc1 Lgv%=\n"
      "Lpv%=:\n"
      "s_nop 3\n"
      "s_cmp_eq_u32 %[sc], 1.0\n"
      "s_cbranch_scc1 Lqv%=\n"
      "v_mul_f32 v16, %[sc], v16\n"
      "v_mul_f32 v17, %[sc], v17\n"
      "v_mul_f32 v18, %[sc], v18\n"
      "v_mul_f32 v19, %[sc], v19\n"
      "v_mul_f32 v20, %[sc], v20\n"
      "v_mul_f32 v21, %[sc], v21\n"
      "v_mul_f32 v22, %[sc], v22\n"
      "v_mul_f32 v23, %[sc], v23\n"
      "v_mul_f32 v24, %[sc], v24\n"
      "v_mul_f32 v25, %[sc], v25\n"
      "v_mul_f32 v26, %[sc], v26\n"
      "v_mul_f32 v27, %[sc], v27\n"
      "v_mul_f32 v28, %[sc], v28\n"
      "v_mul_f32 v29, %[sc], v29\n"
      "v_mul_f32 v30, %[sc], v30\n"
      "v_mul_f32 v31, %[sc], v31\n"
      "v_mul_f32 v32, %[sc], v32\n"
      "v_mul_f32 v33, %[sc], v33\n"
      "v_mul_f32 v34, %[sc], v34\n"
      "v_mul_f32 v35, %[sc], v35\n"
      "v_mul_f32 v36, %[sc], v36\n"
      "v_mul_f32 v37, %[sc], v37\n"
      "v_mul_f32 v38, %[sc], v38\n"
      "v_mul_f32 v39, %[sc], v39\n"
      "v_mul_f32 v40, %[sc], v40\n"
      "v_mul_f32 v41, %[sc], v41\n"
      "v_mul_f32 v42, %[sc], v42\n"
      "v_mul_f32 v43, %[sc], v43\n"
      "v_mul_f32 v44, %[sc], v44\n"
      "v_mul_f32 v45, %[sc], v45\n"
      "v_mul_f32 v46, %[sc], v46\n"
      "v_mul_f32 v47, %[sc], v47\n"
      "s_mov_b32 %[sc], 1.0\n"
      "Lqv%=:\n"
      "v_cvt_pk_bf16_f32 v144, v16, v17\n"
      "v_cvt_pk_bf16_f32 v145, v18, v19\n"
      "v_cvt_pk_bf16_f32 v146, v20, v21\n"
      "v_cvt_pk_bf16_f32 v147, v22, v23\n"
      "v_cvt_pk_bf16_f32 v148, v24, v25\n"
      "v_cvt_pk_bf16_f32 v149, v26, v27\n"
      "v_cvt_pk_bf16_f32 v150, v28, v29\n"
      "v_cvt_pk_bf16_f32 v151, v30, v31\n"
      "v_cvt_pk_bf16_f32 v152, v32, v33\n"
      "v_cvt_pk_bf16_f32 v153, v34, v35\n"
      "v_cvt_pk_bf16_f32 v154, v36, v37\n"
      "v_cvt_pk_bf16_f32 v155, v38, v39\n"
      "v_cvt_pk_bf16_f32 v156, v40, v41\n"
      "v_cvt_pk_bf16_f32 v157, v42, v43\n"
      "v_cvt_pk_bf16_f32 v158, v44, v45\n"
      "v_cvt_pk_bf16_f32 v159, v46, v47\n"
      "s_cmp_eq_u32 %[cnt], 0\n"
      "s_cbranch_scc1 Lz%=\n"
      "s_mov_b64 %[xt], %[xt2]\n"
      "s_mov_b64 vcc, -1\n"
      "s_mov_b32 %[st], 2\n"
      "s_waitcnt vmcnt(0) lgkmcnt(0)\n"
      "global_store_dwordx4 v164, v[144:147], %[sb0] sc1\n"
      "global_store_dwordx4 v164, v[148:151], %[sb1] sc1\n"
      "s_branch Lgv%=\n"
      "Lfv%=:\n"
      "s_mov_b64 vcc, 0\n"
      "v_mfma_f32_16x16x32_bf16 v[16:19], v[48:51], v[64:67], 0\n"
      "ds_read_b64_tr_b16 v[112:113], v162 offset:0\n"
      "v_mfma_f32_16x16x32_bf16 v[20:23], v[52:55], v[64:67], 0\n"
      "ds_read_b64_tr_b16 v[114:115], v162 offset:1024\n"
      "v_mfma_f32_16x16x32_bf16 v[24:27], v[56:59], v[64:67], 0\n"
      "ds_read_b128 v[96:99], v160 offset:0\n"
      "v_mfma_f32_16x16x32_bf16 v[28:31], v[60:63], v[64:67], 0\n"
      "ds_read_b128 v[100:103], v160 offset:2048\n"
      "v_mfma_f32_16x16x32_bf16 v[32:35], v[48:51], v[68:71], 0\n"
      "ds_read_b128 v[104:107], v160 offset:4096\n"
      "v_mfma_f32_16x16x32_bf16 v[36:39], v[52:55], v[68:71], 0\n"
      "ds_read_b128 v[108:111], v160 offset:6144\n"
      "v_mfma_f32_16x16x32_bf16 v[40:43], v[56:59], v[68:71], 0\n"
      "ds_read_b64_tr_b16 v[116:117], v163 offset:0\n"
      "v_mfma_f32_16x16x32_bf16 v[44:47], v[60:63], v[68:71], 0\n"
      "ds_read_b64_tr_b16 v[118:119], v163 offset:1024\n"
      "s_branch Lrv%=\n"
      "Lhv%=:\n"
      "s_cmp_lg_u32 %[st], 2\n"
      "s_cbranch_scc1 Lh1v%=\n"
      "global_store_dwordx4 v165, v[152:155], %[sb0] sc1\n"
      "global_store_dwordx4 v165, v[156:159], %[sb1] sc1\n"
      "s_mov_b32 %[st], 1\n"
      "s_branch Liv%=\n"
      "Lh1v%=:\n"
      "s_getreg_b32 %[tmp], hwreg(HW_REG_IB_STS, 0, 4)\n"
      "s_cmp_le_u32 %[cnt], 3\n"
      "s_cselect_b32 %[tmp], 0, %[tmp]\n"
      "s_cmp_lg_u32 %[tmp], 0\n"
      "s_cbranch_scc1 Liv%=\n"
      "v_mov_b32 v146, %[ta]\n"
      "ds_read_b64 v[144:145], v146\n"
      "v_mov_b32 v147, 0\n"
      "s_waitcnt vmcnt(0) lgkmcnt(0)\n"
      "s_mov_b64 exec, 1\n"
      "global_store_dwordx2 v147, v[144:145], %[fl] sc1\n"
      "s_mov_b64 exec, -1\n"
      "s_mov_b32 %[st], 0\n"
      "s_branch Liv%=\n"
      "Ljv%=:\n"
      "s_getreg_b32 %[tmp], hwreg(HW_REG_IB_STS, 0, 4)\n"
      "s_cmp_le_u32 %[cnt], 3\n"
      "s_cselect_b32 %[tmp], 0, %[tmp]\n"
      "s_cmp_lg_u32 %[tmp], 0\n"
      "s_cbranch_scc1 Lkv%=\n"
      "v_mov_b32 v146, %[ta]\n"
      "ds_read_b64 v[144:145], v146\n"
      "v_mov_b32 v147, 0\n"
      "s_waitcnt vmcnt(0) lgkmcnt(0)\n"
      "s_mov_b64 exec, 1\n"
      "global_store_dwordx2 v147, v[144:145], %[fl] sc1\n"
      "s_mov_b64 exec, -1\n"
      "s_mov_b32 %[st], 0\n"
      "s_branch Lkv%=\n"
      "Lz%=:\n"
      "s_setprio 2\n"
      "s_waitcnt lgkmcnt(0)\n"
      "global_store_dwordx4 v164, v[144:147], %[sa0] sc1\n"
      "v_mov_b32 v64, 0\n"
      "v_mov_b32 v65, 0\n"
      "v_mov_b32 v66, 0\n"
      "v_mov_b32 v67, 0\n"
      "v_mov_b32 v68, 0\n"
      "v_mov_b32 v69, 0\n"
      "v_mov_b32 v70, 0\n"
      "v_mov_b32 v71, 0\n"
      "v_mov_b32 v72, 0\n"
      "v_mov_b32 v73, 0\n"
      "global_store_dwordx4 v164, v[148:151], %[sa1] sc1\n"
      "v_mov_b32 v74, 0\n"
      "v_mov_b32 v75, 0\n"
      "v_mov_b32 v76, 0\n"
      "v_mov_b32 v77, 0\n"
      "v_mov_b32 v78, 0\n"
      "v_mov_b32 v79, 0\n"
      "v_mov_b32 v80, 0\n"
      "v_mov_b32 v81, 0\n"
      "v_mov_b32 v82, 0\n"
      "v_mov_b32 v83, 0\n"
      "global_store_dwordx4 v165, v[152:155], %[sa0] sc1\n"
      "v_mov_b32 v84, 0\n"
      "v_mov_b32 v85, 0\n"
      "v_mov_b32 v86, 0\n"
      "v_mov_b32 v87, 0\n"
      "v_mov_b32 v88, 0\n"
      "v_mov_b32 v89, 0\n"
      "v_mov_b32 v90, 0\n"
      "v_mov_b32 v91, 0\n"
      "v_mov_b32 v92, 0\n"
      "v_mov_b32 v93, 0\n"
      "global_store_dwordx4 v165, v[156:159], %[sa1] sc1\n"
      "v_mov_b32 v94, 0\n"
      "v_mov_b32 v95, 0\n"
      "v_mov_b32 v100, 0\n"
      "v_mov_b32 v101, 0\n"
      "v_mov_b32 v112, 0\n"
      "v_mov_b32 v113, 0\n"
      "v_mov_b32 v114, 0\n"
      "v_mov_b32 v115, 0\n"
      : GEMM_OPS);
  } else {
    LANES;
    const int a = (idx - 30) * 8 + xcd;
    myflag = ws + WS_FW + (a * 8 + wid) * 8;
    if (tid >= 480) {
      const unsigned o1 = (lane & 1) ? 0u : 0x3F803F80u;
      st16(ws + ((lane & 16) ? WS_KVB : WS_KVA) + ((lane >> 1) & 7) * 8 * TS + TS_C + a * 32 + (lane & 1) * 16, u32x4{o1, o1, o1, o1});
    }
    if (wid < 4) {
      const int hd = a * 2 + (wid >> 1), m = wid & 1;
      const int hh = (r >> 2) & 1, reg = (r & 3) + 4 * (r >> 3);
      bf16x8 v;
#pragma unroll
      for (int j = 0; j < 8; ++j) {
        const int d = m == 0 ? 8 * h + j : 16 + j;
        const bool ok = reg < 10 && (m == 0 || (h == 0 && j < 4));
        const u16 w = wphi[hd * 400 + (ok ? d * 20 + 10 * hh + reg : 0)];
        v[j] = ok ? (short)w : (short)0;
      }
      st16(ws + WS_PHI + hd * 2048 + m * 1024 + lane * 16, __builtin_bit_cast(u32x4, v));
    }
    WAIT_BAR(0);
    {
      const lds_cptr wr_ = (lds_cptr)lds + (l16 >> 2) * 640 + (l16 & 3) * 8;
      const int oc = a >> 2;
      char* wd = ws + WS_W + (size_t)((a >> 1) & 1) * 4 * 204800 + (a & 1) * 102400 + (oc >> 1) * 51200 + lane * 16;
#pragma nounroll
      for (int e = -1; e < 4; ++e) {
        if (e >= 0) LBAR();
        const int row0 = e >= 0 ? (2 * e + (g4 >> 1)) * 20 + 8 * (g4 & 1) : 160 + 8 * g4, j = e >= 0 ? (oc & 1) * 4 + e : 8 + (oc & 1);
#pragma nounroll
        for (int f = (wid + 4 * e) & 7; f < 20; f += 8) {
          const int cq = f / 5, n = f - cq * 5;
          const lds_cptr p = wr_ + row0 * 640 + cq * 160 + n * 32;
          st16(wd + cq * 204800 + (j * 5 + n) * 1024, __builtin_bit_cast(u32x4, cat8(ldtr(p), ldtr(p + 4 * 640))));
        }
      }
    }
    {
      const int rw = (xcd * 2 + idx - 30) * 128 + wid * 16;
      float sum[4] = {};
#pragma unroll
      for (int c = 0; c < 5; ++c) {
        const bf16x8 gv = *(const bf16x8*)(gw + (c * 16 + l16) * 8);
#pragma unroll
        for (int it = 0; it < 4; ++it) {
          const bf16x8 xv = *(const bf16x8*)(x + (rw + it * 4 + g4) * D + (c * 16 + l16) * 8);
#pragma unroll
          for (int j = 0; j < 8; ++j) sum[it] += b2f((u16)xv[j]) * b2f((u16)gv[j]);
        }
      }
#pragma unroll
      for (int it = 0; it < 4; ++it)
#pragma unroll
        for (int o = 1; o < 16; o <<= 1) sum[it] += __shfl_xor(sum[it], o);
      const float sv = l16 == 0 ? sum[0] : l16 == 1 ? sum[1] : l16 == 2 ? sum[2] : sum[3];
      if (l16 < 4) st4(ws + WS_G + (rw + l16 * 4 + g4) * 4, __float_as_uint(1.f / (1.f + __expf(-sv))));
    }
    LBAR();
    o0 = o1 = f32x16{};
    z0 = u32x2{};
    z1 = u32x4{};
  }
  {
  u32x8 q1;
  u32x4 q2;
  u64 bad;
  {
    int cnt, sl, tmp, sp;
    int snd = 1;
    asm volatile(
      "v_mov_b32 v133, %[sy]\n"
      "v_mov_b32 v134, %[pgm]\n"
      "v_mov_b32 v136, %[pgp]\n"
      "ds_read_b64 v[138:139], v133 offset:160\n"
      "s_mov_b32 %[sp], 65536\n"
      "Lz%=:\n"
      "ds_read_b32 v135, v133\n"
      "s_sub_u32 %[sp], %[sp], 1\n"
      "s_waitcnt lgkmcnt(0)\n"
      "v_cmp_gt_u32 vcc, 4, v135\n"
      "s_cbranch_vccz Ly%=\n"
      "s_sleep 1\n"
      "s_cmp_lg_u32 %[sp], 0\n"
      "s_cbranch_scc1 Lz%=\n"
      "Ly%=:\n"
      "ds_read_b64 v[104:105], v1\n"
      "ds_read_b64 v[106:107], v1 offset:256\n"
      "ds_read2_b64 v[116:119], v4 offset1:32\n"
      "ds_read2_b64 v[120:123], v5 offset1:32\n"
      "ds_read_b64 v[124:125], v2\n"
      "ds_read_b64 v[126:127], v6\n"
      "ds_read_b64 v[128:129], v7\n"
      "ds_read_b128 v[108:111], v3\n"
      "v_add_u32 v133, 4, v133\n"
      "s_waitcnt lgkmcnt(5)\n"
      "v_mfma_f32_32x32x16_bf16 v[32:47], v[104:107], v[116:119], 0\n"
      "s_waitcnt lgkmcnt(4)\n"
      "v_mfma_f32_32x32x16_bf16 v[48:63], v[104:107], v[120:123], 0\n"
      "s_waitcnt lgkmcnt(2)\n"
      "v_mfma_f32_32x32x8_bf16 v[144:159], v[124:125], v[126:127], v[32:47]\n"
      "s_waitcnt lgkmcnt(1)\n"
      "v_mfma_f32_32x32x8_bf16 v[48:63], v[124:125], v[128:129], v[48:63]\n"
      "s_nop 9\n"
      "v_max3_f32 v4, v144, v145, v146\n"
      "v_max3_f32 v4, v4, v147, v148\n"
      "v_max3_f32 v5, v48, v49, v50\n"
      "v_max3_f32 v4, v4, v149, v150\n"
      "v_max3_f32 v5, v5, v51, v52\n"
      "v_max3_f32 v4, v4, v151, v152\n"
      "v_max3_f32 v5, v5, v53, v54\n"
      "v_max3_f32 v4, v4, v153, v154\n"
      "v_max3_f32 v5, v5, v55, v56\n"
      "v_max3_f32 v4, v4, v155, v156\n"
      "v_max3_f32 v5, v5, v57, v58\n"
      "v_max3_f32 v4, v4, v157, v158\n"
      "v_max3_f32 v5, v5, v59, v60\n"
      "v_max_f32 v4, v4, v159\n"
      "v_max3_f32 v5, v5, v61, v62\n"
      "v_max_f32 v5, v5, v63\n"
      "s_nop 1\n"
      "v_permlane32_swap_b32 v4, v5\n"
      "s_nop 1\n"
      "v_max_f32 v4, v4, v5\n"
      "v_cvt_pk_bf16_f32 v4, -v4, 0\n"
      "v_mov_b32 v5, v4\n"
      "s_nop 1\n"
      "v_permlane32_swap_b32 v4, v5\n"
      "s_mov_b32 exec_lo, 0\n"
      "s_nop 0\n"
      "v_mov_b32 v126, v4\n"
      "v_mov_b32 v128, v5\n"
      "s_mov_b32 exec_lo, -1\n"
      "v_mov_b32 v60, 0\n"
      "v_mov_b32 v61, 0\n"
      "v_mov_b32 v62, 0\n"
      "v_mov_b32 v63, 0\n"
      "v_mfma_f32_32x32x8_bf16 v[32:47], v[124:125], v[126:127], v[32:47]\n"
      "s_cmp_eq_u32 %[pr], 0\n"
      "s_setprio 0\n"
      "s_cbranch_scc1 Lo%=\n"
      "s_setprio 1\n"
      "Lo%=:\n"
      "s_mov_b32 %[cnt], 8\n"
      "s_mov_b32 %[sl], -21504\n"
      "s_waitcnt lgkmcnt(0)\n"
      "s_nop 5\n"
      "s_mov_b64 vcc, -1\n"
      "La%=:\n"
      "v_cvt_pk_bf16_f32 v102, v60, v61\n"
      "v_cvt_pk_bf16_f32 v103, v62, v63\n"
      "v_mfma_f32_32x32x16_bf16 v[48:63], v[104:107], v[120:123], 0\n"
      "ds_read_b64 v[104:105], v1 offset:2688\n"
      "ds_read_b64 v[106:107], v1 offset:2944\n"
      "v_exp_f32 v32, v32\n"
      "v_exp_f32 v33, v33\n"
      "v_exp_f32 v34, v34\n"
      "v_exp_f32 v35, v35\n"
      "v_mfma_f32_32x32x16_bf16 v[80:95], v[112:115], v[100:103], v[80:95]\n"
      "ds_read_b128 v[112:115], v3 offset:704\n"
      "v_cvt_pk_bf16_f32 v96, v32, v33\n"
      "v_cvt_pk_bf16_f32 v97, v34, v35\n"
      "v_exp_f32 v36, v36\n"
      "v_exp_f32 v37, v37\n"
      "v_exp_f32 v38, v38\n"
      "v_exp_f32 v39, v39\n"
      "v_mfma_f32_32x32x8_bf16 v[48:63], v[124:125], v[128:129], v[48:63]\n"
      "ds_read_b64 v[124:125], v2 offset:2688\n"
      "v_cvt_pk_bf16_f32 v98, v36, v37\n"
      "v_cvt_pk_bf16_f32 v99, v38, v39\n"
      "v_exp_f32 v40, v40\n"
      "v_exp_f32 v41, v41\n"
      "v_exp_f32 v42, v42\n"
      "v_exp_f32 v43, v43\n"
      "s_waitcnt lgkmcnt(4)\n"
      "v_mfma_f32_32x32x16_bf16 v[64:79], v[108:111], v[96:99], v[64:79]\n"
      "v_exp_f32 v44, v44\n"
      "v_exp_f32 v45, v45\n"
      "v_exp_f32 v46, v46\n"
      "v_exp_f32 v47, v47\n"
      "v_cvt_pk_bf16_f32 v100, v40, v41\n"
      "v_cvt_pk_bf16_f32 v101, v42, v43\n"
      "s_cbranch_vccnz Lu0%=\n"
      "Lv0%=:\n"
      "v_cvt_pk_bf16_f32 v102, v44, v45\n"
      "v_cvt_pk_bf16_f32 v103, v46, v47\n"
      "s_waitcnt lgkmcnt(1)\n"
      "v_mfma_f32_32x32x16_bf16 v[32:47], v[104:107], v[116:119], 0\n"
      "v_exp_f32 v48, v48\n"
      "v_exp_f32 v49, v49\n"
      "v_exp_f32 v50, v50\n"
      "v_exp_f32 v51, v51\n"
      "v_mfma_f32_32x32x16_bf16 v[64:79], v[112:115], v[100:103], v[64:79]\n"
      "v_cvt_pk_bf16_f32 v96, v48, v49\n"
      "v_cvt_pk_bf16_f32 v97, v50, v51\n"
      "v_exp_f32 v52, v52\n"
      "v_exp_f32 v53, v53\n"
      "v_exp_f32 v54, v54\n"
      "v_exp_f32 v55, v55\n"
      "s_waitcnt lgkmcnt(0)\n"
      "v_mfma_f32_32x32x8_bf16 v[32:47], v[124:125], v[126:127], v[32:47]\n"
      "v_cvt_pk_bf16_f32 v98, v52, v53\n"
      "v_cvt_pk_bf16_f32 v99, v54, v55\n"
      "v_exp_f32 v56, v56\n"
      "v_exp_f32 v57, v57\n"
      "v_exp_f32 v58, v58\n"
      "v_exp_f32 v59, v59\n"
      "v_mfma_f32_32x32x16_bf16 v[80:95], v[108:111], v[96:99], v[80:95]\n"
      "ds_read_b128 v[108:111], v3 offset:2688\n"
      "v_exp_f32 v60, v60\n"
      "v_exp_f32 v61, v61\n"
      "v_exp_f32 v62, v62\n"
      "v_exp_f32 v63, v63\n"
      "v_cvt_pk_bf16_f32 v100, v56, v57\n"
      "v_cvt_pk_bf16_f32 v101, v58, v59\n"
      "Lv1%=:\n"
      "v_cvt_pk_bf16_f32 v102, v60, v61\n"
      "v_cvt_pk_bf16_f32 v103, v62, v63\n"
      "v_mfma_f32_32x32x16_bf16 v[48:63], v[104:107], v[120:123], 0\n"
      "ds_read_b64 v[104:105], v1 offset:5376\n"
      "ds_read_b64 v[106:107], v1 offset:5632\n"
      "v_exp_f32 v32, v32\n"
      "v_exp_f32 v33, v33\n"
      "v_exp_f32 v34, v34\n"
      "v_exp_f32 v35, v35\n"
      "v_mfma_f32_32x32x16_bf16 v[80:95], v[112:115], v[100:103], v[80:95]\n"
      "ds_read_b128 v[112:115], v3 offset:3392\n"
      "v_cvt_pk_bf16_f32 v96, v32, v33\n"
      "v_cvt_pk_bf16_f32 v97, v34, v35\n"
      "v_exp_f32 v36, v36\n"
      "v_exp_f32 v37, v37\n"
      "v_exp_f32 v38, v38\n"
      "v_exp_f32 v39, v39\n"
      "v_mfma_f32_32x32x8_bf16 v[48:63], v[124:125], v[128:129], v[48:63]\n"
      "ds_read_b64 v[124:125], v2 offset:5376\n"
      "v_cvt_pk_bf16_f32 v98, v36, v37\n"
      "v_cvt_pk_bf16_f32 v99, v38, v39\n"
      "v_exp_f32 v40, v40\n"
      "v_exp_f32 v41, v41\n"
      "v_exp_f32 v42, v42\n"
      "v_exp_f32 v43, v43\n"
      "s_waitcnt lgkmcnt(4)\n"
      "v_mfma_f32_32x32x16_bf16 v[64:79], v[108:111], v[96:99], v[64:79]\n"
      "v_exp_f32 v44, v44\n"
      "v_exp_f32 v45, v45\n"
      "v_exp_f32 v46, v46\n"
      "v_exp_f32 v47, v47\n"
      "v_cvt_pk_bf16_f32 v100, v40, v41\n"
      "v_cvt_pk_bf16_f32 v101, v42, v43\n"
      "s_cbranch_vccnz Lu2%=\n"
      "Lv2%=:\n"
      "v_cvt_pk_bf16_f32 v102, v44, v45\n"
      "v_cvt_pk_bf16_f32 v103, v46, v47\n"
      "s_waitcnt lgkmcnt(1)\n"
      "v_mfma_f32_32x32x16_bf16 v[32:47], v[104:107], v[116:119], 0\n"
      "v_exp_f32 v48, v48\n"
      "v_exp_f32 v49, v49\n"
      "v_exp_f32 v50, v50\n"
      "v_exp_f32 v51, v51\n"
      "v_mfma_f32_32x32x16_bf16 v[64:79], v[112:115], v[100:103], v[64:79]\n"
      "v_cvt_pk_bf16_f32 v96, v48, v49\n"
      "v_cvt_pk_bf16_f32 v97, v50, v51\n"
      "v_exp_f32 v52, v52\n"
      "v_exp_f32 v53, v53\n"
      "v_exp_f32 v54, v54\n"
      "v_exp_f32 v55, v55\n"
      "s_waitcnt lgkmcnt(0)\n"
      "v_mfma_f32_32x32x8_bf16 v[32:47], v[124:125], v[126:127], v[32:47]\n"
      "v_cvt_pk_bf16_f32 v98, v52, v53\n"
      "v_cvt_pk_bf16_f32 v99, v54, v55\n"
      "v_exp_f32 v56, v56\n"
      "v_exp_f32 v57, v57\n"
      "v_exp_f32 v58, v58\n"
      "v_exp_f32 v59, v59\n"
      "v_mfma_f32_32x32x16_bf16 v[80:95], v[108:111], v[96:99], v[80:95]\n"
      "ds_read_b128 v[108:111], v3 offset:5376\n"
      "v_exp_f32 v60, v60\n"
      "v_exp_f32 v61, v61\n"
      "v_exp_f32 v62, v62\n"
      "v_exp_f32 v63, v63\n"
      "v_cvt_pk_bf16_f32 v100, v56, v57\n"
      "v_cvt_pk_bf16_f32 v101, v58, v59\n"
      "Lv3%=:\n"
      "v_cvt_pk_bf16_f32 v102, v60, v61\n"
      "v_cvt_pk_bf16_f32 v103, v62, v63\n"
      "v_mfma_f32_32x32x16_bf16 v[48:63], v[104:107], v[120:123], 0\n"
      "ds_read_b64 v[104:105], v1 offset:8064\n"
      "ds_read_b64 v[106:107], v1 offset:8320\n"
      "v_exp_f32 v32, v32\n"
      "v_exp_f32 v33, v33\n"
      "v_exp_f32 v34, v34\n"
      "v_exp_f32 v35, v35\n"
      "v_mfma_f32_32x32x16_bf16 v[80:95], v[112:115], v[100:103], v[80:95]\n"
      "ds_read_b128 v[112:115], v3 offset:6080\n"
      "v_cvt_pk_bf16_f32 v96, v32, v33\n"
      "v_cvt_pk_bf16_f32 v97, v34, v35\n"
      "v_exp_f32 v36, v36\n"
      "v_exp_f32 v37, v37\n"
      "v_exp_f32 v38, v38\n"
      "v_exp_f32 v39, v39\n"
      "v_mfma_f32_32x32x8_bf16 v[48:63], v[124:125], v[128:129], v[48:63]\n"
      "ds_read_b64 v[124:125], v2 offset:8064\n"
      "v_cvt_pk_bf16_f32 v98, v36, v37\n"
      "v_cvt_pk_bf16_f32 v99, v38, v39\n"
      "v_exp_f32 v40, v40\n"
      "v_exp_f32 v41, v41\n"
      "v_exp_f32 v42, v42\n"
      "v_exp_f32 v43, v43\n"
      "s_waitcnt lgkmcnt(4)\n"
      "v_mfma_f32_32x32x16_bf16 v[64:79], v[108:111], v[96:99], v[64:79]\n"
      "v_exp_f32 v44, v44\n"
      "v_exp_f32 v45, v45\n"
      "v_exp_f32 v46, v46\n"
      "v_exp_f32 v47, v47\n"
      "v_cvt_pk_bf16_f32 v100, v40, v41\n"
      "v_cvt_pk_bf16_f32 v101, v42, v43\n"
      "s_cbranch_vccnz Lu4%=\n"
      "Lv4%=:\n"
      "ds_read_b32 v135, v133\n"
      "ds_read_b32 v137, v136\n"
      "ds_read_b32 v6, v12\n"
      "ds_read_b32 v7, v11\n"
      "v_cvt_pk_bf16_f32 v102, v44, v45\n"
      "v_cvt_pk_bf16_f32 v103, v46, v47\n"
      "s_waitcnt lgkmcnt(5)\n"
      "v_mfma_f32_32x32x16_bf16 v[32:47], v[104:107], v[116:119], 0\n"
      "v_exp_f32 v48, v48\n"
      "v_exp_f32 v49, v49\n"
      "v_exp_f32 v50, v50\n"
      "v_exp_f32 v51, v51\n"
      "v_mfma_f32_32x32x16_bf16 v[64:79], v[112:115], v[100:103], v[64:79]\n"
      "v_cvt_pk_bf16_f32 v96, v48, v49\n"
      "v_cvt_pk_bf16_f32 v97, v50, v51\n"
      "v_exp_f32 v52, v52\n"
      "v_exp_f32 v53, v53\n"
      "v_exp_f32 v54, v54\n"
      "v_exp_f32 v55, v55\n"
      "s_waitcnt lgkmcnt(4)\n"
      "v_mfma_f32_32x32x8_bf16 v[32:47], v[124:125], v[126:127], v[32:47]\n"
      "v_cvt_pk_bf16_f32 v98, v52, v53\n"
      "v_cvt_pk_bf16_f32 v99, v54, v55\n"
      "v_exp_f32 v56, v56\n"
      "v_exp_f32 v57, v57\n"
      "v_exp_f32 v58, v58\n"
      "v_exp_f32 v59, v59\n"
      "v_mfma_f32_32x32x16_bf16 v[80:95], v[108:111], v[96:99], v[80:95]\n"
      "ds_read_b128 v[108:111], v3 offset:8064\n"
      "v_exp_f32 v60, v60\n"
      "v_exp_f32 v61, v61\n"
      "v_exp_f32 v62, v62\n"
      "v_exp_f32 v63, v63\n"
      "v_cvt_pk_bf16_f32 v100, v56, v57\n"
      "v_cvt_pk_bf16_f32 v101, v58, v59\n"
      "s_cmp_eq_u32 %[cnt], 6\n"
      "s_cselect_b32 %[tmp], 107520, %[sl]\n"
      "s_waitcnt lgkmcnt(1)\n"
      "v_cmp_ne_u32 vcc, 4, v135\n"
      "s_cbranch_vccnz Lw%=\n"
      "Lc%=:\n"
      "v_cmp_ge_u32 vcc, v137, v133\n"
      "s_setprio 0\n"
      "s_cbranch_vccz Lp%=\n"
      "s_setprio 1\n"
      "Lp%=:\n"
      "s_cmp_lg_u32 %[snd], 0\n"
      "s_cselect_b64 vcc, -1, 0\n"
      "v_cvt_pk_bf16_f32 v102, v60, v61\n"
      "v_cvt_pk_bf16_f32 v103, v62, v63\n"
      "v_add_u32 v1, %[tmp], v1\n"
      "v_add_u32 v2, %[tmp], v2\n"
      "v_mfma_f32_32x32x16_bf16 v[48:63], v[104:107], v[120:123], 0\n"
      "ds_read_b64 v[104:105], v1 offset:0\n"
      "ds_read_b64 v[106:107], v1 offset:256\n"
      "v_exp_f32 v32, v32\n"
      "v_exp_f32 v33, v33\n"
      "v_exp_f32 v34, v34\n"
      "v_exp_f32 v35, v35\n"
      "v_mfma_f32_32x32x16_bf16 v[80:95], v[112:115], v[100:103], v[80:95]\n"
      "ds_read_b128 v[112:115], v3 offset:8768\n"
      "s_mov_b64 exec, 1\n"
      "ds_write_b32 v134, v133\n"
      "s_mov_b64 exec, -1\n"
      "v_add_u32 v133, 4, v133\n"
      "v_add_u32 v3, %[tmp], v3\n"
      "v_cvt_pk_bf16_f32 v96, v32, v33\n"
      "v_cvt_pk_bf16_f32 v97, v34, v35\n"
      "v_exp_f32 v36, v36\n"
      "v_exp_f32 v37, v37\n"
      "v_exp_f32 v38, v38\n"
      "v_exp_f32 v39, v39\n"
      "v_mfma_f32_32x32x8_bf16 v[48:63], v[124:125], v[128:129], v[48:63]\n"
      "ds_read_b64 v[124:125], v2 offset:0\n"
      "v_cvt_pk_bf16_f32 v98, v36, v37\n"
      "v_cvt_pk_bf16_f32 v99, v38, v39\n"
      "v_exp_f32 v40, v40\n"
      "v_exp_f32 v41, v41\n"
      "v_exp_f32 v42, v42\n"
      "v_exp_f32 v43, v43\n"
      "s_waitcnt lgkmcnt(5)\n"
      "v_mfma_f32_32x32x16_bf16 v[64:79], v[108:111], v[96:99], v[64:79]\n"
      "v_exp_f32 v44, v44\n"
      "v_exp_f32 v45, v45\n"
      "v_exp_f32 v46, v46\n"
      "v_exp_f32 v47, v47\n"
      "v_cvt_pk_bf16_f32 v100, v40, v41\n"
      "v_cvt_pk_bf16_f32 v101, v42, v43\n"
      "s_cbranch_vccnz Lu6%=\n"
      "Lv6%=:\n"
      "v_cvt_pk_bf16_f32 v102, v44, v45\n"
      "v_cvt_pk_bf16_f32 v103, v46, v47\n"
      "s_waitcnt lgkmcnt(2)\n"
      "v_mfma_f32_32x32x16_bf16 v[32:47], v[104:107], v[116:119], 0\n"
      "v_exp_f32 v48, v48\n"
      "v_exp_f32 v49, v49\n"
      "v_exp_f32 v50, v50\n"
      "v_exp_f32 v51, v51\n"
      "v_mfma_f32_32x32x16_bf16 v[64:79], v[112:115], v[100:103], v[64:79]\n"
      "v_cvt_pk_bf16_f32 v96, v48, v49\n"
      "v_cvt_pk_bf16_f32 v97, v50, v51\n"
      "v_exp_f32 v52, v52\n"
      "v_exp_f32 v53, v53\n"
      "v_exp_f32 v54, v54\n"
      "v_exp_f32 v55, v55\n"
      "s_waitcnt lgkmcnt(0)\n"
      "v_mfma_f32_32x32x8_bf16 v[32:47], v[124:125], v[126:127], v[32:47]\n"
      "v_cvt_pk_bf16_f32 v98, v52, v53\n"
      "v_cvt_pk_bf16_f32 v99, v54, v55\n"
      "v_exp_f32 v56, v56\n"
      "v_exp_f32 v57, v57\n"
      "v_exp_f32 v58, v58\n"
      "v_exp_f32 v59, v59\n"
      "v_mfma_f32_32x32x16_bf16 v[80:95], v[108:111], v[96:99], v[80:95]\n"
      "ds_read_b128 v[108:111], v3 offset:0\n"
      "v_exp_f32 v60, v60\n"
      "v_exp_f32 v61, v61\n"
      "v_exp_f32 v62, v62\n"
      "v_exp_f32 v63, v63\n"
      "v_cvt_pk_bf16_f32 v100, v56, v57\n"
      "v_cvt_pk_bf16_f32 v101, v58, v59\n"
      "Lv7%=:\n"
      "s_sub_i32 %[cnt], %[cnt], 1\n"
      "s_cmp_lg_u32 %[cnt], 0\n"
      "s_cbranch_scc1 La%=\n"
      "s_branch Le%=\n"
      "Lw%=:\n"
      "s_mov_b32 %[sp], 65536\n"
      "Lx%=:\n"
      "s_sleep 1\n"
      "ds_read_b32 v135, v133\n"
      "s_sub_i32 %[sp], %[sp], 1\n"
      "s_waitcnt lgkmcnt(0)\n"
      "v_cmp_ne_u32 vcc, 4, v135\n"
      "s_cbranch_vccz Lc%=\n"
      "s_cmp_lg_u32 %[sp], 0\n"
      "s_cbranch_scc1 Lx%=\n"
      "s_branch Lc%=\n"
      "Lu7%=:\n"
      "s_getreg_b32 %[sp], hwreg(HW_REG_IB_STS, 0, 4)\n"
      "s_cmp_le_u32 %[cnt], 7\n"
      "s_cselect_b32 %[sp], 0, %[sp]\n"
      "s_cmp_lg_u32 %[sp], 0\n"
      "s_cbranch_scc1 Lv7%=\n"
      "s_waitcnt vmcnt(0)\n"
      "s_mov_b64 exec, 1\n"
      "global_store_dwordx2 v134, v[138:139], %[fa] sc1\n"
      "ds_write_b32 v134, v133 offset:64\n"
      "s_mov_b64 exec, -1\n"
      "s_mov_b32 %[snd], 0\n"
      "s_mov_b64 vcc, 0\n"
      "s_waitcnt lgkmcnt(0)\n"
      "s_branch Lv7%=\n"
      "Lu6%=:\n"
      "s_getreg_b32 %[sp], hwreg(HW_REG_IB_STS, 0, 4)\n"
      "s_cmp_le_u32 %[cnt], 7\n"
      "s_cselect_b32 %[sp], 0, %[sp]\n"
      "s_cmp_lg_u32 %[sp], 0\n"
      "s_cbranch_scc1 Lv6%=\n"
      "s_waitcnt vmcnt(0)\n"
      "s_mov_b64 exec, 1\n"
      "global_store_dwordx2 v134, v[138:139], %[fa] sc1\n"
      "ds_write_b32 v134, v133 offset:64\n"
      "s_mov_b64 exec, -1\n"
      "s_mov_b32 %[snd], 0\n"
      "s_mov_b64 vcc, 0\n"
      "s_waitcnt lgkmcnt(0)\n"
      "s_branch Lv6%=\n"
      "Lu4%=:\n"
      "s_getreg_b32 %[sp], hwreg(HW_REG_IB_STS, 0, 4)\n"
      "s_cmp_le_u32 %[cnt], 7\n"
      "s_cselect_b32 %[sp], 0, %[sp]\n"
      "s_cmp_lg_u32 %[sp], 0\n"
      "s_cbranch_scc1 Lv4%=\n"
      "s_waitcnt vmcnt(0)\n"
      "s_mov_b64 exec, 1\n"
      "global_store_dwordx2 v134, v[138:139], %[fa] sc1\n"
      "ds_write_b32 v134, v133 offset:64\n"
      "s_mov_b64 exec, -1\n"
      "s_mov_b32 %[snd], 0\n"
      "s_mov_b64 vcc, 0\n"
      "s_waitcnt lgkmcnt(0)\n"
      "s_branch Lv4%=\n"
      "Lu3%=:\n"
      "s_getreg_b32 %[sp], hwreg(HW_REG_IB_STS, 0, 4)\n"
      "s_cmp_le_u32 %[cnt], 7\n"
      "s_cselect_b32 %[sp], 0, %[sp]\n"
      "s_cmp_lg_u32 %[sp], 0\n"
      "s_cbranch_scc1 Lv3%=\n"
      "s_waitcnt vmcnt(0)\n"
      "s_mov_b64 exec, 1\n"
      "global_store_dwordx2 v134, v[138:139], %[fa] sc1\n"
      "ds_write_b32 v134, v133 offset:64\n"
      "s_mov_b64 exec, -1\n"
      "s_mov_b32 %[snd], 0\n"
      "s_mov_b64 vcc, 0\n"
      "s_waitcnt lgkmcnt(0)\n"
      "s_branch Lv3%=\n"
      "Lu2%=:\n"
      "s_getreg_b32 %[sp], hwreg(HW_REG_IB_STS, 0, 4)\n"
      "s_cmp_le_u32 %[cnt], 7\n"
      "s_cselect_b32 %[sp], 0, %[sp]\n"
      "s_cmp_lg_u32 %[sp], 0\n"
      "s_cbranch_scc1 Lv2%=\n"
      "s_waitcnt vmcnt(0)\n"
      "s_mov_b64 exec, 1\n"
      "global_store_dwordx2 v134, v[138:139], %[fa] sc1\n"
      "ds_write_b32 v134, v133 offset:64\n"
      "s_mov_b64 exec, -1\n"
      "s_mov_b32 %[snd], 0\n"
      "s_mov_b64 vcc, 0\n"
      "s_waitcnt lgkmcnt(0)\n"
      "s_branch Lv2%=\n"
      "Lu1%=:\n"
      "s_getreg_b32 %[sp], hwreg(HW_REG_IB_STS, 0, 4)\n"
      "s_cmp_le_u32 %[cnt], 7\n"
      "s_cselect_b32 %[sp], 0, %[sp]\n"
      "s_cmp_lg_u32 %[sp], 0\n"
      "s_cbranch_scc1 Lv1%=\n"
      "s_waitcnt vmcnt(0)\n"
      "s_mov_b64 exec, 1\n"
      "global_store_dwordx2 v134, v[138:139], %[fa] sc1\n"
      "ds_write_b32 v134, v133 offset:64\n"
      "s_mov_b64 exec, -1\n"
      "s_mov_b32 %[snd], 0\n"
      "s_mov_b64 vcc, 0\n"
      "s_waitcnt lgkmcnt(0)\n"
      "s_branch Lv1%=\n"
      "Lu0%=:\n"
      "s_getreg_b32 %[sp], hwreg(HW_REG_IB_STS, 0, 4)\n"
      "s_cmp_le_u32 %[cnt], 7\n"
      "s_cselect_b32 %[sp], 0, %[sp]\n"
      "s_cmp_lg_u32 %[sp], 0\n"
      "s_cbranch_scc1 Lv0%=\n"
      "s_waitcnt vmcnt(0)\n"
      "s_mov_b64 exec, 1\n"
      "global_store_dwordx2 v134, v[138:139], %[fa] sc1\n"
      "ds_write_b32 v134, v133 offset:64\n"
      "s_mov_b64 exec, -1\n"
      "s_mov_b32 %[snd], 0\n"
      "s_mov_b64 vcc, 0\n"
      "s_waitcnt lgkmcnt(0)\n"
      "s_branch Lv0%=\n"
      "Lm%=:\n"
      "s_mov_b32 %[sp], 65536\n"
      "Ln%=:\n"
      "ds_read_b32 v6, v12\n"
      "ds_read_b32 v7, v11\n"
      "s_sub_u32 %[sp], %[sp], 1\n"
      "s_waitcnt lgkmcnt(0)\n"
      "v_cmp_lt_u32 vcc, v6, v13\n"
      "s_cbranch_vccz Lq%=\n"
      "s_sleep 1\n"
      "s_cmp_lg_u32 %[sp], 0\n"
      "s_cbranch_scc1 Ln%=\n"
      "s_branch Lq%=\n"
      "Le%=:\n"
      "v_cvt_pk_bf16_f32 v102, v60, v61\n"
      "v_cvt_pk_bf16_f32 v103, v62, v63\n"
      "v_cmp_lt_u32 vcc, v6, v13\n"
      "s_nop 0\n"
      "v_mfma_f32_32x32x16_bf16 v[80:95], v[112:115], v[100:103], v[80:95]\n"
      "s_cbranch_vccnz Lm%=\n"
      "Lq%=:\n"
      "s_mov_b32 exec_lo, 0\n"
      "v_mov_b32 v72, v76\n"
      "v_lshlrev_b32 v73, 16, v4\n"
      "s_mov_b32 exec_lo, -1\n"
      "ds_write_b128 v8, v[64:67]\n"
      "ds_write_b128 v8, v[68:71] offset:1024\n"
      "ds_write_b128 v8, v[72:75] offset:2048\n"
      "s_mov_b64 exec, 1\n"
      "ds_write_b32 v134, v133\n"
      "s_mov_b64 exec, -1\n"
      "s_mov_b32 %[sp], 65536\n"
      "v_lshlrev_b32 v98, 16, v5\n"
      "v_sub_f32 v7, 1.0, v7\n"
      "Lr%=:\n"
      "ds_read_b32 v137, v136\n"
      "ds_read_b64 v[96:97], v10\n"
      "ds_read_b128 v[100:103], v9\n"
      "ds_read_b128 v[104:107], v9 offset:1024\n"
      "ds_read_b128 v[108:111], v9 offset:2048\n"
      "s_sub_u32 %[sp], %[sp], 1\n"
      "s_waitcnt lgkmcnt(4)\n"
      "v_cmp_gt_u32 vcc, v133, v137\n"
      "s_cselect_b64 vcc, 0, vcc\n"
      "s_cbranch_vccnz Lr%=\n"
      "s_waitcnt lgkmcnt(3)\n"
      "v_min_f32 v99, v98, v97\n"
      "v_max_f32 v114, v92, v96\n"
      "v_sub_f32 v112, v99, v98\n"
      "v_sub_f32 v113, v99, v97\n"
      "v_exp_f32 v112, v112\n"
      "v_exp_f32 v113, v113\n"
      "v_cmp_nlt_f32 %[bad], v114, %[big]\n"
      "v_mul_f32 v99, v92, v112\n"
      "v_fmac_f32 v99, v96, v113\n"
      "v_rcp_f32 v114, v99\n"
      "v_cmp_ngt_f32 vcc, v99, %[tiny]\n"
      "s_or_b64 %[bad], %[bad], vcc\n"
      "v_mul_f32 v114, v114, v7\n"
      "v_mul_f32 v112, v112, v114\n"
      "v_mul_f32 v114, v113, v114\n"
      "s_waitcnt lgkmcnt(0)\n"
      "v_pk_mul_f32 v[100:101], v[100:101], v[114:115] op_sel_hi:[1,0]\n"
      "v_pk_mul_f32 v[102:103], v[102:103], v[114:115] op_sel_hi:[1,0]\n"
      "v_pk_mul_f32 v[104:105], v[104:105], v[114:115] op_sel_hi:[1,0]\n"
      "v_pk_mul_f32 v[106:107], v[106:107], v[114:115] op_sel_hi:[1,0]\n"
      "v_pk_mul_f32 v[108:109], v[108:109], v[114:115] op_sel_hi:[1,0]\n"
      "v_pk_mul_f32 v[110:111], v[110:111], v[114:115] op_sel_hi:[1,0]\n"
      "v_pk_fma_f32 v[100:101], v[80:81], v[112:113], v[100:101] op_sel_hi:[1,0,1]\n"
      "v_pk_fma_f32 v[102:103], v[82:83], v[112:113], v[102:103] op_sel_hi:[1,0,1]\n"
      "v_pk_fma_f32 v[104:105], v[84:85], v[112:113], v[104:105] op_sel_hi:[1,0,1]\n"
      "v_pk_fma_f32 v[106:107], v[86:87], v[112:113], v[106:107] op_sel_hi:[1,0,1]\n"
      "v_pk_fma_f32 v[108:109], v[88:89], v[112:113], v[108:109] op_sel_hi:[1,0,1]\n"
      "v_pk_fma_f32 v[110:111], v[90:91], v[112:113], v[110:111] op_sel_hi:[1,0,1]\n"
      "v_pk_add_f32 v[96:97], v[100:101], v[102:103]\n"
      "v_pk_add_f32 v[98:99], v[104:105], v[106:107]\n"
      "v_pk_add_f32 v[112:113], v[108:109], v[110:111]\n"
      "v_cvt_pk_bf16_f32 v64, v100, v101\n"
      "v_cvt_pk_bf16_f32 v65, v102, v103\n"
      "v_cvt_pk_bf16_f32 v66, v104, v105\n"
      "v_pk_add_f32 v[96:97], v[96:97], v[98:99]\n"
      "v_cvt_pk_bf16_f32 v67, v106, v107\n"
      "v_cvt_pk_bf16_f32 v68, v108, v109\n"
      "v_cvt_pk_bf16_f32 v69, v110, v111\n"
      "v_pk_add_f32 v[96:97], v[96:97], v[112:113]\n"
      "v_min_u32 v64, -2, v64\n"
      "v_min_u32 v65, -2, v65\n"
      "v_min_u32 v66, -2, v66\n"
      "v_min_u32 v67, -2, v67\n"
      "v_add_f32 v96, v96, v97\n"
      "v_min_u32 v68, -2, v68\n"
      "v_min_u32 v69, -2, v69\n"
      "v_cmp_nlt_f32 vcc, |v96|, %[huge]\n"
      "s_or_b64 %[bad], %[bad], vcc\n"
      "s_cbranch_scc1 Lt%=\n"
      "global_store_dwordx4 v14, v[64:67], %[ob] sc1\n"
      "s_mov_b32 exec_hi, 0\n"
      "global_store_dwordx2 v15, v[68:69], %[ob] sc1\n"
      "s_mov_b32 exec_hi, -1\n"
      "Lt%=:\n"
      "s_nop 0\n"
      :
        "={v[116:123]}"(q1), "={v[126:129]}"(q2), "+{v[0:15]}"(at), "+{v[64:79]}"(o0), "+{v[80:95]}"(o1), "+{v[100:101]}"(z0), "+{v[112:115]}"(z1),
        [cnt] "=&s"(cnt), [sl] "=&s"(sl), [tmp] "=&s"(tmp), [sp] "=&s"(sp), [snd] "+s"(snd), [bad] "=&s"(bad)
      : [sy] "s"(sy), [pgm] "s"(pgm), [pgp] "s"(pgp), [fa] "s"(uni(myflag - pgm)), [pr] "s"(kh), [ob] "s"(ws + WS_OB),
        [big] "s"(1e30f), [tiny] "s"(1e-20f), [huge] "s"(3e38f)
      : "v32", "v33", "v34", "v35", "v36", "v37", "v38", "v39", "v40", "v41", "v42", "v43", "v44", "v45", "v46", "v47", "v48", "v49", "v50", "v51", "v52", "v53", "v54", "v55", "v56", "v57", "v58", "v59", "v60", "v61", "v62", "v63", "v96", "v97", "v98", "v99", "v102", "v103", "v104", "v105", "v106", "v107", "v108", "v109", "v110", "v111", "v133", "v134", "v135", "v136", "v137", "v138", "v139", "v144", "v145", "v146", "v147", "v148", "v149", "v150", "v151", "v152", "v153", "v154", "v155", "v156", "v157", "v158", "v159", "v124", "v125", "s88", "s89", "s90", "s91", "s94", "s95", "s96", "s97", "s98", "s99", "vcc", "scc", "memory");
  }
  if (__builtin_expect(bad != 0, 0)) {
    LANES;
    const bf16x8 qq1 = BF8(__builtin_shufflevector(q1, q1, 4, 5, 6, 7));
    const u32x2 qq2 = {h ? 0u : q2[2], q2[3]};
    f32x16 oo = {};
    float m = -INFINITY;
#pragma nounroll
    for (int t = 0; t < 64; ++t) {
      const char* tb = kvg + t * TS + head * TQ;
      const int c = vslot >> 2;
      const char* vb = vslot < 20 ? kvg + t * TS + TS_V + h * 10240 + (head * 20 + ((c == 1 ? 2 : c == 2 ? 1 : c) << 2) + (vslot & 3)) * 16 : kvg + TS_C + (head >> 1) * 32 + (vslot - 20) * 16;
      const char* vc = vslot < 20 ? vb + 20480 : vb;
      const bf16x8 w0 = cat8(ld8c(vb), ld8c(vb + 8)), w1 = cat8(ld8c(vc), ld8c(vc + 8));
      f32x16 s = MFMA32(gfrag1(tb, posk, h), qq1, f32x16{});
      s = MFMA8(gfrag2(tb, posk), BF4(qq2), s);
      const float mn = __builtin_fmaxf(m, rowmax16(s)), al = __builtin_amdgcn_exp2f(m - mn);
      m = mn;
#pragma unroll
      for (int i = 0; i < 16; ++i) { oo[i] *= al; s[i] = __builtin_amdgcn_exp2f(s[i] - mn); }
      const u32x4 pa = {pk2(s[0], s[1]), pk2(s[2], s[3]), pk2(s[4], s[5]), pk2(s[6], s[7])};
      const u32x4 pb = {pk2(s[8], s[9]), pk2(s[10], s[11]), pk2(s[12], s[13]), pk2(s[14], s[15])};
      oo = MFMA32(w0, BF8(pa), oo);
      oo = MFMA32(w1, BF8(pb), oo);
    }
    const float sc2 = __uint_as_float(at[7]) / oo[12];
    unsigned w[6];
#pragma unroll
    for (int j = 0; j < 6; ++j) w[j] = __builtin_elementwise_min(pk2(oo[2 * j] * sc2, oo[2 * j + 1] * sc2), 0xFFFFFFFEu);
    store_o(ws + WS_OB, own, head, u32x4{w[0], w[1], w[2], w[3]}, u32x2{w[4], w[5]}, r, h);
  }
  {
    LANES;
    const u32x4 pa = {lds0 + (kh ? L_W1 : L_W0) + lane * 16, (unsigned)(kh * 8192 + 4096 + lane * 16), (unsigned)(16384 + kh * 2048 + g4 * 256 + l16 * 8), lds0 + L_APART + qp * 5120 + lane * 16};
    const u32x8 pb = {at[8], at[9], lds0 + (lane ? SY_LPROG + qp * 4 : SY_WLAND), lane ? 1u : 12u, (unsigned)(l16 * D * 2 + g4 * 8), pgm, pgp, sy + 40};
    int sp, mk;
    u64 rt;
    asm volatile(
      "v_mov_b32 v133, 1\n"
      "s_mov_b64 exec, 1\n"
      "ds_add_u32 v162, v133\n"
      "s_mov_b64 exec, -1\n"
      "s_mov_b32 %[sp], %[cap]\n"
      "La%=:\n"
      "ds_read_b32 v132, v162\n"
      "s_sub_u32 %[sp], %[sp], 1\n"
      "s_waitcnt lgkmcnt(0)\n"
      "v_cmp_lt_u32 vcc, v132, v163\n"
      "s_cselect_b64 vcc, 0, vcc\n"
      "s_cbranch_vccnz La%=\n"
      "global_load_dwordx4 v[52:55], v157, %[src] offset:-4096 sc1\n"
      "global_load_dwordx4 v[56:59], v157, %[src] offset:-3072 sc1\n"
      "global_load_dwordx4 v[60:63], v157, %[src] offset:-2048 sc1\n"
      "global_load_dwordx4 v[64:67], v157, %[src] offset:-1024 sc1\n"
      "global_load_dwordx4 v[68:71], v157, %[src] offset:0 sc1\n"
      "global_load_dwordx4 v[72:75], v157, %[src] offset:1024 sc1\n"
      "global_load_dwordx4 v[76:79], v157, %[src] offset:2048 sc1\n"
      "global_load_dwordx4 v[80:83], v157, %[src] offset:3072 sc1\n"
      "global_load_dwordx2 v[84:85], v158, %[src] offset:0 sc1\n"
      "global_load_dwordx2 v[86:87], v158, %[src] offset:128 sc1\n"
      "global_load_dwordx2 v[88:89], v158, %[src] offset:1024 sc1\n"
      "global_load_dwordx2 v[90:91], v158, %[src] offset:1152 sc1\n"
      "v_and_b32 v134, 8, v164\n"
      "v_and_b32 v135, -9, v164\n"
      "v_lshl_add_u32 v134, v134, 2, v135\n"
      "s_cmp_eq_u32 %[kh], 0\n"
      "s_cbranch_scc0 Lb%=\n"
      "ds_read_b128 v[32:35], v159 offset:0\n"
      "ds_read_b128 v[36:39], v159 offset:1024\n"
      "ds_read_b128 v[40:43], v159 offset:2048\n"
      "v_mov_b32 v44, 0\n"
      "v_mov_b32 v45, 0\n"
      "v_mov_b32 v46, 0\n"
      "v_mov_b32 v47, 0\n"
      "v_mov_b32 v48, 0\n"
      "v_mov_b32 v49, 0\n"
      "v_mov_b32 v50, 0\n"
      "v_mov_b32 v51, 0\n"
      "s_branch Lc%=\n"
      "Lb%=:\n"
      "ds_read_b128 v[44:47], v159 offset:3072\n"
      "ds_read_b128 v[48:51], v159 offset:4096\n"
      "v_mov_b32 v32, 0\n"
      "v_mov_b32 v33, 0\n"
      "v_mov_b32 v34, 0\n"
      "v_mov_b32 v35, 0\n"
      "v_mov_b32 v36, 0\n"
      "v_mov_b32 v37, 0\n"
      "v_mov_b32 v38, 0\n"
      "v_mov_b32 v39, 0\n"
      "v_mov_b32 v40, 0\n"
      "v_mov_b32 v41, 0\n"
      "v_mov_b32 v42, 0\n"
      "v_mov_b32 v43, 0\n"
      "Lc%=:\n"
      "s_mov_b32 %[mk], -1\n"
      "s_mov_b32 %[sp], %[cap]\n"
      "ds_read_b128 v[92:95], v156 offset:0\n"
      "ds_read_b128 v[96:99], v156 offset:1024\n"
      "ds_read_b128 v[100:103], v156 offset:2048\n"
      "ds_read_b128 v[104:107], v156 offset:3072\n"
      "ds_read_b128 v[108:111], v156 offset:4096\n"
      "ds_read_b128 v[112:115], v156 offset:5120\n"
      "ds_read_b128 v[116:119], v156 offset:6144\n"
      "ds_read_b128 v[120:123], v156 offset:7168\n"
      "ds_read_b128 v[124:127], v156 offset:8192\n"
      "ds_read_b128 v[128:131], v156 offset:9216\n"
      "ds_read_b128 v[136:139], v156 offset:10240\n"
      "ds_read_b128 v[140:143], v156 offset:11264\n"
      "ds_read_b128 v[144:147], v156 offset:12288\n"
      "ds_read_b128 v[148:151], v156 offset:13312\n"
      "ds_read_b128 v[152:155], v156 offset:14336\n"
      "ds_read_b128 v[16:19], v156 offset:15360\n"
      "ds_read_b128 v[20:23], v156 offset:16384\n"
      "ds_read_b128 v[24:27], v156 offset:17408\n"
      "ds_read_b128 v[28:31], v156 offset:18432\n"
      "ds_read_b128 v[0:3], v156 offset:19456\n"
      "ds_read_b128 v[4:7], v156 offset:20480\n"
      "ds_read_b128 v[8:11], v156 offset:21504\n"
      "Lg0%=:\n"
      "s_waitcnt vmcnt(11)\n"
      "v_max3_u32 v132, v52, v53, v54\n"
      "s_bitcmp1_b32 %[mk], 0\n"
      "v_max_u32 v132, v132, v55\n"
      "s_cbranch_scc0 Lk0%=\n"
      "v_cmp_eq_u32 vcc, -1, v132\n"
      "s_cbranch_vccnz Lc0%=\n"
      "Lk0%=:\n"
      "ds_read_b128 v[12:15], v156 offset:22528\n"
      "s_waitcnt lgkmcnt(15)\n"
      "v_mfma_f32_16x16x32_bf16 v[32:35], v[92:95], v[52:55], v[32:35]\n"
      "ds_read_b128 v[92:95], v156 offset:23552\n"
      "s_waitcnt lgkmcnt(15)\n"
      "v_mfma_f32_16x16x32_bf16 v[36:39], v[96:99], v[52:55], v[36:39]\n"
      "ds_read_b128 v[96:99], v156 offset:24576\n"
      "s_waitcnt lgkmcnt(15)\n"
      "v_mfma_f32_16x16x32_bf16 v[40:43], v[100:103], v[52:55], v[40:43]\n"
      "ds_read_b128 v[100:103], v156 offset:25600\n"
      "s_waitcnt lgkmcnt(15)\n"
      "v_mfma_f32_16x16x32_bf16 v[44:47], v[104:107], v[52:55], v[44:47]\n"
      "ds_read_b128 v[104:107], v156 offset:26624\n"
      "s_waitcnt lgkmcnt(15)\n"
      "v_mfma_f32_16x16x32_bf16 v[48:51], v[108:111], v[52:55], v[48:51]\n"
      "Lg1%=:\n"
      "s_waitcnt vmcnt(10)\n"
      "v_max3_u32 v132, v56, v57, v58\n"
      "s_bitcmp1_b32 %[mk], 1\n"
      "v_max_u32 v132, v132, v59\n"
      "s_cbranch_scc0 Lk1%=\n"
      "v_cmp_eq_u32 vcc, -1, v132\n"
      "s_cbranch_vccnz Lc1%=\n"
      "Lk1%=:\n"
      "ds_read_b128 v[108:111], v156 offset:27648\n"
      "s_waitcnt lgkmcnt(15)\n"
      "v_mfma_f32_16x16x32_bf16 v[32:35], v[112:115], v[56:59], v[32:35]\n"
      "ds_read_b128 v[112:115], v156 offset:28672\n"
      "s_waitcnt lgkmcnt(15)\n"
      "v_mfma_f32_16x16x32_bf16 v[36:39], v[116:119], v[56:59], v[36:39]\n"
      "ds_read_b128 v[116:119], v156 offset:29696\n"
      "s_waitcnt lgkmcnt(15)\n"
      "v_mfma_f32_16x16x32_bf16 v[40:43], v[120:123], v[56:59], v[40:43]\n"
      "ds_read_b128 v[120:123], v156 offset:30720\n"
      "s_waitcnt lgkmcnt(15)\n"
      "v_mfma_f32_16x16x32_bf16 v[44:47], v[124:127], v[56:59], v[44:47]\n"
      "ds_read_b128 v[124:127], v156 offset:31744\n"
      "s_waitcnt lgkmcnt(15)\n"
      "v_mfma_f32_16x16x32_bf16 v[48:51], v[128:131], v[56:59], v[48:51]\n"
      "Lg2%=:\n"
      "s_waitcnt vmcnt(9)\n"
      "v_max3_u32 v132, v60, v61, v62\n"
      "s_bitcmp1_b32 %[mk], 2\n"
      "v_max_u32 v132, v132, v63\n"
      "s_cbranch_scc0 Lk2%=\n"
      "v_cmp_eq_u32 vcc, -1, v132\n"
      "s_cbranch_vccnz Lc2%=\n"
      "Lk2%=:\n"
      "ds_read_b128 v[128:131], v156 offset:32768\n"
      "s_waitcnt lgkmcnt(15)\n"
      "v_mfma_f32_16x16x32_bf16 v[32:35], v[136:139], v[60:63], v[32:35]\n"
      "ds_read_b128 v[136:139], v156 offset:33792\n"
      "s_waitcnt lgkmcnt(15)\n"
      "v_mfma_f32_16x16x32_bf16 v[36:39], v[140:143], v[60:63], v[36:39]\n"
      "ds_read_b128 v[140:143], v156 offset:34816\n"
      "s_waitcnt lgkmcnt(15)\n"
      "v_mfma_f32_16x16x32_bf16 v[40:43], v[144:147], v[60:63], v[40:43]\n"
      "ds_read_b128 v[144:147], v156 offset:35840\n"
      "s_waitcnt lgkmcnt(15)\n"
      "v_mfma_f32_16x16x32_bf16 v[44:47], v[148:151], v[60:63], v[44:47]\n"
      "ds_read_b128 v[148:151], v156 offset:36864\n"
      "s_waitcnt lgkmcnt(15)\n"
      "v_mfma_f32_16x16x32_bf16 v[48:51], v[152:155], v[60:63], v[48:51]\n"
      "Lg3%=:\n"
      "s_waitcnt vmcnt(8)\n"
      "v_max3_u32 v132, v64, v65, v66\n"
      "s_bitcmp1_b32 %[mk], 3\n"
      "v_max_u32 v132, v132, v67\n"
      "s_cbranch_scc0 Lk3%=\n"
      "v_cmp_eq_u32 vcc, -1, v132\n"
      "s_cbranch_vccnz Lc3%=\n"
      "Lk3%=:\n"
      "ds_read_b128 v[152:155], v156 offset:37888\n"
      "s_waitcnt lgkmcnt(15)\n"
      "v_mfma_f32_16x16x32_bf16 v[32:35], v[16:19], v[64:67], v[32:35]\n"
      "ds_read_b128 v[16:19], v156 offset:38912\n"
      "s_waitcnt lgkmcnt(15)\n"
      "v_mfma_f32_16x16x32_bf16 v[36:39], v[20:23], v[64:67], v[36:39]\n"
      "ds_read_b128 v[20:23], v156 offset:39936\n"
      "s_waitcnt lgkmcnt(15)\n"
      "v_mfma_f32_16x16x32_bf16 v[40:43], v[24:27], v[64:67], v[40:43]\n"
      "ds_read_b128 v[24:27], v156 offset:40960\n"
      "s_waitcnt lgkmcnt(15)\n"
      "v_mfma_f32_16x16x32_bf16 v[44:47], v[28:31], v[64:67], v[44:47]\n"
      "ds_read_b128 v[28:31], v156 offset:41984\n"
      "s_waitcnt lgkmcnt(15)\n"
      "v_mfma_f32_16x16x32_bf16 v[48:51], v[0:3], v[64:67], v[48:51]\n"
      "Lg4%=:\n"
      "s_waitcnt vmcnt(7)\n"
      "v_max3_u32 v132, v68, v69, v70\n"
      "s_bitcmp1_b32 %[mk], 4\n"
      "v_max_u32 v132, v132, v71\n"
      "s_cbranch_scc0 Lk4%=\n"
      "v_cmp_eq_u32 vcc, -1, v132\n"
      "s_cbranch_vccnz Lc4%=\n"
      "Lk4%=:\n"
      "ds_read_b128 v[0:3], v156 offset:43008\n"
      "s_waitcnt lgkmcnt(15)\n"
      "v_mfma_f32_16x16x32_bf16 v[32:35], v[4:7], v[68:71], v[32:35]\n"
      "ds_read_b128 v[4:7], v156 offset:44032\n"
      "s_waitcnt lgkmcnt(15)\n"
      "v_mfma_f32_16x16x32_bf16 v[36:39], v[8:11], v[68:71], v[36:39]\n"
      "ds_read_b128 v[8:11], v156 offset:45056\n"
      "s_waitcnt lgkmcnt(15)\n"
      "v_mfma_f32_16x16x32_bf16 v[40:43], v[12:15], v[68:71], v[40:43]\n"
      "ds_read_b128 v[12:15], v156 offset:46080\n"
      "s_waitcnt lgkmcnt(15)\n"
      "v_mfma_f32_16x16x32_bf16 v[44:47], v[92:95], v[68:71], v[44:47]\n"
      "ds_read_b128 v[92:95], v156 offset:47104\n"
      "s_waitcnt lgkmcnt(15)\n"
      "v_mfma_f32_16x16x32_bf16 v[48:51], v[96:99], v[68:71], v[48:51]\n"
      "Lg5%=:\n"
      "s_waitcnt vmcnt(6)\n"
      "v_max3_u32 v132, v72, v73, v74\n"
      "s_bitcmp1_b32 %[mk], 5\n"
      "v_max_u32 v132, v132, v75\n"
      "s_cbranch_scc0 Lk5%=\n"
      "v_cmp_eq_u32 vcc, -1, v132\n"
      "s_cbranch_vccnz Lc5%=\n"
      "Lk5%=:\n"
      "ds_read_b128 v[96:99], v156 offset:48128\n"
      "s_waitcnt lgkmcnt(15)\n"
      "v_mfma_f32_16x16x32_bf16 v[32:35], v[100:103], v[72:75], v[32:35]\n"
      "ds_read_b128 v[100:103], v156 offset:49152\n"
      "s_waitcnt lgkmcnt(15)\n"
      "v_mfma_f32_16x16x32_bf16 v[36:39], v[104:107], v[72:75], v[36:39]\n"
      "ds_read_b128 v[104:107], v156 offset:50176\n"
      "s_waitcnt lgkmcnt(15)\n"
      "v_mfma_f32_16x16x32_bf16 v[40:43], v[108:111], v[72:75], v[40:43]\n"
      "s_waitcnt lgkmcnt(15)\n"
      "v_mfma_f32_16x16x32_bf16 v[44:47], v[112:115], v[72:75], v[44:47]\n"
      "s_waitcnt lgkmcnt(15)\n"
      "v_mfma_f32_16x16x32_bf16 v[48:51], v[116:119], v[72:75], v[48:51]\n"
      "Lg6%=:\n"
      "s_waitcnt vmcnt(5)\n"
      "v_max3_u32 v132, v76, v77, v78\n"
      "s_bitcmp1_b32 %[mk], 6\n"
      "v_max_u32 v132, v132, v79\n"
      "s_cbranch_scc0 Lk6%=\n"
      "v_cmp_eq_u32 vcc, -1, v132\n"
      "s_cbranch_vccnz Lc6%=\n"
      "Lk6%=:\n"
      "s_waitcnt lgkmcnt(15)\n"
      "v_mfma_f32_16x16x32_bf16 v[32:35], v[120:123], v[76:79], v[32:35]\n"
      "s_waitcnt lgkmcnt(15)\n"
      "v_mfma_f32_16x16x32_bf16 v[36:39], v[124:127], v[76:79], v[36:39]\n"
      "s_waitcnt lgkmcnt(15)\n"
      "v_mfma_f32_16x16x32_bf16 v[40:43], v[128:131], v[76:79], v[40:43]\n"
      "s_waitcnt lgkmcnt(15)\n"
      "v_mfma_f32_16x16x32_bf16 v[44:47], v[136:139], v[76:79], v[44:47]\n"
      "s_waitcnt lgkmcnt(15)\n"
      "v_mfma_f32_16x16x32_bf16 v[48:51], v[140:143], v[76:79], v[48:51]\n"
      "Lg7%=:\n"
      "s_waitcnt vmcnt(4)\n"
      "v_max3_u32 v132, v80, v81, v82\n"
      "s_bitcmp1_b32 %[mk], 7\n"
      "v_max_u32 v132, v132, v83\n"
      "s_cbranch_scc0 Lk7%=\n"
      "v_cmp_eq_u32 vcc, -1, v132\n"
      "s_cbranch_vccnz Lc7%=\n"
      "Lk7%=:\n"
      "s_waitcnt lgkmcnt(14)\n"
      "v_mfma_f32_16x16x32_bf16 v[32:35], v[144:147], v[80:83], v[32:35]\n"
      "s_waitcnt lgkmcnt(13)\n"
      "v_mfma_f32_16x16x32_bf16 v[36:39], v[148:151], v[80:83], v[36:39]\n"
      "s_waitcnt lgkmcnt(12)\n"
      "v_mfma_f32_16x16x32_bf16 v[40:43], v[152:155], v[80:83], v[40:43]\n"
      "s_waitcnt lgkmcnt(11)\n"
      "v_mfma_f32_16x16x32_bf16 v[44:47], v[16:19], v[80:83], v[44:47]\n"
      "s_waitcnt lgkmcnt(10)\n"
      "v_mfma_f32_16x16x32_bf16 v[48:51], v[20:23], v[80:83], v[48:51]\n"
      "Lg8%=:\n"
      "s_waitcnt vmcnt(2)\n"
      "v_max3_u32 v132, v84, v85, v86\n"
      "s_bitcmp1_b32 %[mk], 8\n"
      "v_max_u32 v132, v132, v87\n"
      "s_cbranch_scc0 Lk8%=\n"
      "v_cmp_eq_u32 vcc, -1, v132\n"
      "s_cbranch_vccnz Lc8%=\n"
      "Lk8%=:\n"
      "s_waitcnt lgkmcnt(9)\n"
      "v_mfma_f32_16x16x32_bf16 v[32:35], v[24:27], v[84:87], v[32:35]\n"
      "s_waitcnt lgkmcnt(8)\n"
      "v_mfma_f32_16x16x32_bf16 v[36:39], v[28:31], v[84:87], v[36:39]\n"
      "s_waitcnt lgkmcnt(7)\n"
      "v_mfma_f32_16x16x32_bf16 v[40:43], v[0:3], v[84:87], v[40:43]\n"
      "s_waitcnt lgkmcnt(6)\n"
      "v_mfma_f32_16x16x32_bf16 v[44:47], v[4:7], v[84:87], v[44:47]\n"
      "s_waitcnt lgkmcnt(5)\n"
      "v_mfma_f32_16x16x32_bf16 v[48:51], v[8:11], v[84:87], v[48:51]\n"
      "Lg9%=:\n"
      "s_waitcnt vmcnt(0)\n"
      "v_max3_u32 v132, v88, v89, v90\n"
      "s_bitcmp1_b32 %[mk], 9\n"
      "v_max_u32 v132, v132, v91\n"
      "s_cbranch_scc0 Lk9%=\n"
      "v_cmp_eq_u32 vcc, -1, v132\n"
      "s_cbranch_vccnz Lc9%=\n"
      "Lk9%=:\n"
      "s_waitcnt lgkmcnt(4)\n"
      "v_mfma_f32_16x16x32_bf16 v[32:35], v[12:15], v[88:91], v[32:35]\n"
      "s_waitcnt lgkmcnt(3)\n"
      "v_mfma_f32_16x16x32_bf16 v[36:39], v[92:95], v[88:91], v[36:39]\n"
      "s_waitcnt lgkmcnt(2)\n"
      "v_mfma_f32_16x16x32_bf16 v[40:43], v[96:99], v[88:91], v[40:43]\n"
      "s_waitcnt lgkmcnt(1)\n"
      "v_mfma_f32_16x16x32_bf16 v[44:47], v[100:103], v[88:91], v[44:47]\n"
      "s_waitcnt lgkmcnt(0)\n"
      "v_mfma_f32_16x16x32_bf16 v[48:51], v[104:107], v[88:91], v[48:51]\n"
      "s_branch Le%=\n"
      "Lc0%=:\n"
      "s_call_b64 %[rt], Lr%=\n"
      "s_branch Lg0%=\n"
      "Lc1%=:\n"
      "s_call_b64 %[rt], Lr%=\n"
      "s_branch Lg1%=\n"
      "Lc2%=:\n"
      "s_call_b64 %[rt], Lr%=\n"
      "s_branch Lg2%=\n"
      "Lc3%=:\n"
      "s_call_b64 %[rt], Lr%=\n"
      "s_branch Lg3%=\n"
      "Lc4%=:\n"
      "s_call_b64 %[rt], Lr%=\n"
      "s_branch Lg4%=\n"
      "Lc5%=:\n"
      "s_call_b64 %[rt], Lr%=\n"
      "s_branch Lg5%=\n"
      "Lc6%=:\n"
      "s_call_b64 %[rt], Lr%=\n"
      "s_branch Lg6%=\n"
      "Lc7%=:\n"
      "s_call_b64 %[rt], Lr%=\n"
      "s_branch Lg7%=\n"
      "Lc8%=:\n"
      "s_call_b64 %[rt], Lr%=\n"
      "s_branch Lg8%=\n"
      "Lc9%=:\n"
      "s_call_b64 %[rt], Lr%=\n"
      "s_branch Lg9%=\n"
      "Lr%=:\n"
      "s_waitcnt vmcnt(0)\n"
      "s_mov_b32 %[mk], 0\n"
      "v_max3_u32 v133, v88, v89, v90\n"
      "v_max_u32 v133, v133, v91\n"
      "v_cmp_eq_u32 vcc, -1, v133\n"
      "s_cmp_lg_u64 vcc, 0\n"
      "s_addc_u32 %[mk], %[mk], %[mk]\n"
      "v_max3_u32 v132, v84, v85, v86\n"
      "v_max_u32 v132, v132, v87\n"
      "v_cmp_eq_u32 vcc, -1, v132\n"
      "s_cmp_lg_u64 vcc, 0\n"
      "s_addc_u32 %[mk], %[mk], %[mk]\n"
      "v_max3_u32 v133, v80, v81, v82\n"
      "v_max_u32 v133, v133, v83\n"
      "v_cmp_eq_u32 vcc, -1, v133\n"
      "s_cmp_lg_u64 vcc, 0\n"
      "s_addc_u32 %[mk], %[mk], %[mk]\n"
      "v_max3_u32 v132, v76, v77, v78\n"
      "v_max_u32 v132, v132, v79\n"
      "v_cmp_eq_u32 vcc, -1, v132\n"
      "s_cmp_lg_u64 vcc, 0\n"
      "s_addc_u32 %[mk], %[mk], %[mk]\n"
      "v_max3_u32 v133, v72, v73, v74\n"
      "v_max_u32 v133, v133, v75\n"
      "v_cmp_eq_u32 vcc, -1, v133\n"
      "s_cmp_lg_u64 vcc, 0\n"
      "s_addc_u32 %[mk], %[mk], %[mk]\n"
      "v_max3_u32 v132, v68, v69, v70\n"
      "v_max_u32 v132, v132, v71\n"
      "v_cmp_eq_u32 vcc, -1, v132\n"
      "s_cmp_lg_u64 vcc, 0\n"
      "s_addc_u32 %[mk], %[mk], %[mk]\n"
      "v_max3_u32 v133, v64, v65, v66\n"
      "v_max_u32 v133, v133, v67\n"
      "v_cmp_eq_u32 vcc, -1, v133\n"
      "s_cmp_lg_u64 vcc, 0\n"
      "s_addc_u32 %[mk], %[mk], %[mk]\n"
      "v_max3_u32 v132, v60, v61, v62\n"
      "v_max_u32 v132, v132, v63\n"
      "v_cmp_eq_u32 vcc, -1, v132\n"
      "s_cmp_lg_u64 vcc, 0\n"
      "s_addc_u32 %[mk], %[mk], %[mk]\n"
      "v_max3_u32 v133, v56, v57, v58\n"
      "v_max_u32 v133, v133, v59\n"
      "v_cmp_eq_u32 vcc, -1, v133\n"
      "s_cmp_lg_u64 vcc, 0\n"
      "s_addc_u32 %[mk], %[mk], %[mk]\n"
      "v_max3_u32 v132, v52, v53, v54\n"
      "v_max_u32 v132, v132, v55\n"
      "v_cmp_eq_u32 vcc, -1, v132\n"
      "s_cmp_lg_u64 vcc, 0\n"
      "s_addc_u32 %[mk], %[mk], %[mk]\n"
      "s_sub_u32 %[sp], %[sp], 1\n"
      "s_cselect_b32 %[mk], 0, %[mk]\n"
      "s_cmp_eq_u32 %[mk], 0\n"
      "s_cbranch_scc1 Lx%=\n"
      "s_bitcmp1_b32 %[mk], 0\n"
      "s_cbranch_scc0 Ln0%=\n"
      "global_load_dwordx4 v[52:55], v157, %[src] offset:-4096 sc1\n"
      "Ln0%=:\n"
      "s_bitcmp1_b32 %[mk], 1\n"
      "s_cbranch_scc0 Ln1%=\n"
      "global_load_dwordx4 v[56:59], v157, %[src] offset:-3072 sc1\n"
      "Ln1%=:\n"
      "s_bitcmp1_b32 %[mk], 2\n"
      "s_cbranch_scc0 Ln2%=\n"
      "global_load_dwordx4 v[60:63], v157, %[src] offset:-2048 sc1\n"
      "Ln2%=:\n"
      "s_bitcmp1_b32 %[mk], 3\n"
      "s_cbranch_scc0 Ln3%=\n"
      "global_load_dwordx4 v[64:67], v157, %[src] offset:-1024 sc1\n"
      "Ln3%=:\n"
      "s_bitcmp1_b32 %[mk], 4\n"
      "s_cbranch_scc0 Ln4%=\n"
      "global_load_dwordx4 v[68:71], v157, %[src] offset:0 sc1\n"
      "Ln4%=:\n"
      "s_bitcmp1_b32 %[mk], 5\n"
      "s_cbranch_scc0 Ln5%=\n"
      "global_load_dwordx4 v[72:75], v157, %[src] offset:1024 sc1\n"
      "Ln5%=:\n"
      "s_bitcmp1_b32 %[mk], 6\n"
      "s_cbranch_scc0 Ln6%=\n"
      "global_load_dwordx4 v[76:79], v157, %[src] offset:2048 sc1\n"
      "Ln6%=:\n"
      "s_bitcmp1_b32 %[mk], 7\n"
      "s_cbranch_scc0 Ln7%=\n"
      "global_load_dwordx4 v[80:83], v157, %[src] offset:3072 sc1\n"
      "Ln7%=:\n"
      "s_bitcmp1_b32 %[mk], 8\n"
      "s_cbranch_scc0 Ln8%=\n"
      "global_load_dwordx2 v[84:85], v158, %[src] offset:0 sc1\n"
      "global_load_dwordx2 v[86:87], v158, %[src] offset:128 sc1\n"
      "Ln8%=:\n"
      "s_bitcmp1_b32 %[mk], 9\n"
      "s_cbranch_scc0 Ln9%=\n"
      "global_load_dwordx2 v[88:89], v158, %[src] offset:1024 sc1\n"
      "global_load_dwordx2 v[90:91], v158, %[src] offset:1152 sc1\n"
      "Ln9%=:\n"
      "Lx%=:\n"
      "s_setpc_b64 %[rt]\n"
      "Le%=:\n"
      "s_mov_b32 %[sp], %[cap]\n"
      "s_cmp_eq_u32 %[kh], 0\n"
      "s_nop 4\n"
      "s_cbranch_scc0 Lu%=\n"
      "ds_write_b128 v160, v[44:47] offset:0\n"
      "ds_write_b128 v160, v[48:51] offset:1024\n"
      "s_mov_b64 exec, 1\n"
      "ds_write_b32 v165, v167\n"
      "s_mov_b64 exec, -1\n"
      "Lv%=:\n"
      "ds_read_b32 v132, v166\n"
      "ds_read_b128 v[52:55], v161 offset:0\n"
      "ds_read_b128 v[56:59], v161 offset:1024\n"
      "ds_read_b128 v[60:63], v161 offset:2048\n"
      "s_sub_u32 %[sp], %[sp], 1\n"
      "s_waitcnt lgkmcnt(3)\n"
      "v_cmp_gt_u32 vcc, v167, v132\n"
      "s_cselect_b64 vcc, 0, vcc\n"
      "s_cbranch_vccnz Lv%=\n"
      "s_waitcnt lgkmcnt(2)\n"
      "v_pk_add_f32 v[32:33], v[32:33], v[52:53]\n"
      "v_pk_add_f32 v[34:35], v[34:35], v[54:55]\n"
      "v_cvt_pk_bf16_f32 v64, v32, v33\n"
      "v_cvt_pk_bf16_f32 v65, v34, v35\n"
      "s_waitcnt lgkmcnt(1)\n"
      "v_pk_add_f32 v[36:37], v[36:37], v[56:57]\n"
      "v_pk_add_f32 v[38:39], v[38:39], v[58:59]\n"
      "v_cvt_pk_bf16_f32 v66, v36, v37\n"
      "v_cvt_pk_bf16_f32 v67, v38, v39\n"
      "s_nop 1\n"
      "v_permlane16_swap_b32 v64, v66\n"
      "v_permlane16_swap_b32 v65, v67\n"
      "s_nop 1\n"
      "global_store_dwordx4 v134, v[64:67], %[out] offset:0 sc1\n"
      "s_waitcnt lgkmcnt(0)\n"
      "v_pk_add_f32 v[40:41], v[40:41], v[60:61]\n"
      "v_pk_add_f32 v[42:43], v[42:43], v[62:63]\n"
      "v_cvt_pk_bf16_f32 v68, v40, v41\n"
      "v_cvt_pk_bf16_f32 v69, v42, v43\n"
      "global_store_dwordx2 v164, v[68:69], %[out] offset:64 sc1\n"
      "s_branch Lz%=\n"
      "Lu%=:\n"
      "ds_write_b128 v160, v[32:35] offset:0\n"
      "ds_write_b128 v160, v[36:39] offset:1024\n"
      "ds_write_b128 v160, v[40:43] offset:2048\n"
      "s_mov_b64 exec, 1\n"
      "ds_write_b32 v165, v167\n"
      "s_mov_b64 exec, -1\n"
      "Lw%=:\n"
      "ds_read_b32 v132, v166\n"
      "ds_read_b128 v[52:55], v161 offset:0\n"
      "ds_read_b128 v[56:59], v161 offset:1024\n"
      "s_sub_u32 %[sp], %[sp], 1\n"
      "s_waitcnt lgkmcnt(2)\n"
      "v_cmp_gt_u32 vcc, v167, v132\n"
      "s_cselect_b64 vcc, 0, vcc\n"
      "s_cbranch_vccnz Lw%=\n"
      "s_waitcnt lgkmcnt(1)\n"
      "v_pk_add_f32 v[44:45], v[44:45], v[52:53]\n"
      "v_pk_add_f32 v[46:47], v[46:47], v[54:55]\n"
      "v_cvt_pk_bf16_f32 v64, v44, v45\n"
      "v_cvt_pk_bf16_f32 v65, v46, v47\n"
      "s_waitcnt lgkmcnt(0)\n"
      "v_pk_add_f32 v[48:49], v[48:49], v[56:57]\n"
      "v_pk_add_f32 v[50:51], v[50:51], v[58:59]\n"
      "v_cvt_pk_bf16_f32 v66, v48, v49\n"
      "v_cvt_pk_bf16_f32 v67, v50, v51\n"
      "s_nop 1\n"
      "v_permlane16_swap_b32 v64, v66\n"
      "v_permlane16_swap_b32 v65, v67\n"
      "s_nop 1\n"
      "global_store_dwordx4 v134, v[64:67], %[out] offset:96 sc1\n"
      "Lz%=:\n"
      :
        [sp] "=&s"(sp), [mk] "=&s"(mk), [rt] "=&s"(rt)
      : "{v[156:159]}"(pa), "{v[160:167]}"(pb), [src] "s"(ws + WS_OB + (size_t)(rb3 * 4 + qp) * 20480), [cap] "s"(SPIN_CAP), [kh] "s"(kh), [out] "s"(out + (size_t)(rb3 * 64 + qp * 16) * D + cb3 * 80)
      : "v0", "v1", "v2", "v3", "v4", "v5", "v6", "v7", "v8", "v9", "v10", "v11", "v12", "v13", "v14", "v15", "v16", "v17", "v18", "v19", "v20", "v21", "v22", "v23", "v24", "v25", "v26", "v27", "v28", "v29", "v30", "v31", "v32", "v33", "v34", "v35", "v36", "v37", "v38", "v39", "v40", "v41", "v42", "v43", "v44", "v45", "v46", "v47", "v48", "v49", "v50", "v51", "v52", "v53", "v54", "v55", "v56", "v57", "v58", "v59", "v60", "v61", "v62", "v63", "v64", "v65", "v66", "v67", "v68", "v69", "v70", "v71", "v72", "v73", "v74", "v75", "v76", "v77", "v78", "v79", "v80", "v81", "v82", "v83", "v84", "v85", "v86", "v87", "v88", "v89", "v90", "v91", "v92", "v93", "v94", "v95", "v96", "v97", "v98", "v99", "v100", "v101", "v102", "v103", "v104", "v105", "v106", "v107", "v108", "v109", "v110", "v111", "v112", "v113", "v114", "v115", "v116", "v117", "v118", "v119", "v120", "v121", "v122", "v123", "v124", "v125", "v126", "v127", "v128", "v129", "v130", "v131", "v132", "v133", "v134", "v135", "v136", "v137", "v138", "v139", "v140", "v141", "v142", "v143", "v144", "v145", "v146", "v147", "v148", "v149", "v150", "v151", "v152", "v153", "v154", "v155", "s88", "s89", "s90", "s91", "s92", "s93", "s94", "s95", "s96", "s97", "s98", "vcc", "scc", "memory");
  }
  }
}

#define WSOFF 0
extern "C" void kernel_launch(void* const* d_in, const int* in_sizes, int n_in, void* d_out, int out_size, void* d_ws, size_t ws_size,
                              hipStream_t stream) {
  (void)hipFuncSetAttribute((const void*)fused, hipFuncAttributeMaxDynamicSharedMemorySize, LDS_BYTES);
  fused<<<256, 768, LDS_BYTES, stream>>>((const u16*)d_in[0], (const u16*)d_in[1], (const u16*)d_in[5], (char*)d_ws + WSOFF, (u16*)d_out,
                                         (const u16*)d_in[3], (const u16*)d_in[6], (const u16*)d_in[2], (const u16*)d_in[7]);
}
```
